```python
import torch

def _softmax(x, axis=-1):
    return torch.softmax(x, dim=axis)
def _gelu(x):
    return torch.nn.functional.gelu(x, approximate="tanh")
def _silu(x):
    return torch.nn.functional.silu(x)
def _sigmoid(x):
    return torch.sigmoid(x)
def _layernorm(x, axis=-1, eps=1e-5):
    m = x.mean(dim=axis, keepdim=True)
    v = x.var(dim=axis, keepdim=True, unbiased=False)
    return (x - m) / torch.sqrt(v + eps)
def _scatter_add(tgt, idx, src, axis=0):
    return tgt.clone().index_add_(axis, idx, src)
def _segment_sum(src, seg, n):
    out = torch.zeros((n,) + src.shape[1:], dtype=src.dtype)
    return out.index_add_(0, seg, src)
def _pad(x, pw):
    flat = tuple(v for pair in reversed(pw) for v in pair)
    return torch.nn.functional.pad(x, flat)

kernel_name = 'parallel_sum__ssm_conv_s4d__dense_activation_relu__prompt'

def setup_inputs():

    _w = (lambda a: (lambda t: t.to(torch.bfloat16) if t.is_floating_point() else t)(torch.as_tensor(a)))
    def _A_setup():
        Bsz, L, D, N = 1, 2048, 512, 128
        return {
            "u": _w((lambda *s: torch.randn(*s, dtype=torch.bfloat16) * 0.05)(Bsz, L, D)),
            "log_A_real": _w((lambda *s: torch.randn(*s, dtype=torch.bfloat16) * 3.2)(D, N) * 0.5),
            "A_imag": _w((lambda *s: torch.randn(*s, dtype=torch.bfloat16) * 0.1)(D, N) * 3.14),
            "C_real": _w((lambda *s: torch.randn(*s, dtype=torch.bfloat16) * 1.6)(D, N) / N ** 0.5),
            "C_imag": _w((lambda *s: torch.randn(*s, dtype=torch.bfloat16) * 1.6)(D, N) / N ** 0.5),
            "log_dt": _w((lambda *s: torch.randn(*s, dtype=torch.bfloat16) * 0.1)(D) * 0.5 - 4.0),
            "D_skip": _w((lambda *s: torch.randn(*s, dtype=torch.bfloat16) * 0.2)(D)),
        }
    _A_in = _A_setup()

    def _B_setup():
        B, S, D, Dff = 1, 2048, 512, 1792
        return {
            "x": _w((lambda *s: torch.randn(*s, dtype=torch.bfloat16) * 0.05)(B, S, D)),
            "w1": _w((lambda *s: torch.randn(*s, dtype=torch.bfloat16) * 0.05)(D, Dff)),
            "w2": _w((lambda *s: torch.randn(*s, dtype=torch.bfloat16) * 0.05)(Dff, D)),
        }
    _B_in = _B_setup()

    return {
        "A_u": _A_in["u"],
        "A_log_A_real": _A_in["log_A_real"],
        "A_A_imag": _A_in["A_imag"],
        "A_C_real": _A_in["C_real"],
        "A_C_imag": _A_in["C_imag"],
        "A_log_dt": _A_in["log_dt"],
        "A_D_skip": _A_in["D_skip"],
        "B_x": _B_in["x"],
        "B_w1": _B_in["w1"],
        "B_w2": _B_in["w2"],
    }

def reference(A_u, A_log_A_real, A_A_imag, A_C_real, A_C_imag, A_log_dt, A_D_skip, B_x, B_w1, B_w2):
    def _A_ref(u, log_A_real, A_imag, C_real, C_imag, log_dt, D_skip):
        L = u.shape[1]
        dt = torch.exp(log_dt)
        A = -torch.exp(log_A_real) + 1j * A_imag
        C = (C_real + 1j * C_imag) * dt[:, None]
        pos = (lambda x, dt: x.to(dt))(torch.arange(L), log_dt.dtype)
        P = torch.exp((A * dt[:, None])[:, :, None] * pos[None, None, :])
        K = 2.0 * torch.einsum("dn,dnl->dl", C, P).real
        L2 = 2 * L
        u_t = (lambda x, *d: x.permute(*d))(u, 0, 2, 1)
        Kf = (lambda x, *a, **k: torch.fft.rfft(x.float(), *a, **k))(K, L2)
        Uf = (lambda x, *a, **k: torch.fft.rfft(x.float(), *a, **k))(u_t, L2)
        y = (lambda x, *a, **k: torch.fft.irfft(x, *a, **k).to(torch.bfloat16))(Uf * Kf[None, :, :], L2)[:, :, :L]
        y = (lambda x, *d: x.permute(*d))(y, 0, 2, 1)
        return y + u * D_skip[None, None, :]

    def _B_ref(x, w1, w2):
        h = torch.einsum("bsd,df->bsf", x, w1)
        zero = torch.zeros_like(h)
        h = torch.maximum(h, zero)
        return torch.einsum("bsf,fd->bsd", h, w2)

    x = A_u
    xn = _layernorm(x, axis=-1)
    return x + _A_ref(xn, A_log_A_real, A_A_imag, A_C_real, A_C_imag, A_log_dt, A_D_skip) + _B_ref(xn, B_w1, B_w2)
```

```cpp
#include <hip/hip_runtime.h>
#include <hip/hip_bf16.h>
#include <stdint.h>
typedef unsigned short u16;
static __device__ __forceinline__ float bf2f(u16 v){ return __uint_as_float(((unsigned)v)<<16); }
static __device__ __forceinline__ u16 f2bf(float f){ __bf16 b = (__bf16)f; return __builtin_bit_cast(u16,b); }
static __device__ __forceinline__ float rbf(float f){ return bf2f(f2bf(f)); }
constexpr int L=2048, D=512, N=128, F=1792;

__global__ void ln_k(const u16* x, u16* xn){
  int row=blockIdx.x; int t=threadIdx.x;
  float v[8]; float s=0;
  for(int j=0;j<8;j++){ v[j]=bf2f(x[row*D+t*8+j]); s+=v[j]; }
  for(int o=32;o>0;o>>=1) s+=__shfl_xor(s,o);
  float mean=s*(1.0f/D);
  float q=0; for(int j=0;j<8;j++){ float d=v[j]-mean; q+=d*d; }
  for(int o=32;o>0;o>>=1) q+=__shfl_xor(q,o);
  float m=rbf(mean), var=rbf(q*(1.0f/D));
  float sd=rbf(sqrtf(rbf(var+1e-5f)));
  for(int j=0;j<8;j++){ xn[row*D+t*8+j]=f2bf(rbf(v[j]-m)/sd); }
}
__global__ void kgen_k(const u16* lar,const u16* aim,const u16* cre,const u16* cim,const u16* ldt,float* K){
  int d=blockIdx.x;
  __shared__ float al[N],be[N],cr[N],ci[N];
  float dt=rbf(expf(bf2f(ldt[d])));
  if(threadIdx.x<N){ int n=threadIdx.x;
    al[n]=-rbf(expf(bf2f(lar[d*N+n])))*dt; be[n]=bf2f(aim[d*N+n])*dt; cr[n]=bf2f(cre[d*N+n])*dt; ci[n]=bf2f(cim[d*N+n])*dt; }
  __syncthreads();
  for(int l=threadIdx.x;l<L;l+=blockDim.x){
    float p=rbf((float)l); float s=0;
    for(int n=0;n<N;n++){ float e=expf(al[n]*p); float sn,cs; sincosf(be[n]*p,&sn,&cs); s+=e*(cr[n]*cs-ci[n]*sn); }
    K[d*L+l]=2.f*s;
  }
}
__global__ void conv_k(const float* K,const u16* xn,const u16* x,const u16* dsk,float* ya){
  int d=blockIdx.x;
  __shared__ float k[L],u[L];
  for(int l=threadIdx.x;l<L;l+=blockDim.x){ k[l]=K[d*L+l]; u[l]=bf2f(xn[l*D+d]); }
  __syncthreads();
  float ds=bf2f(dsk[d]);
  for(int l=threadIdx.x;l<L;l+=blockDim.x){
    float s=0; for(int j=0;j<=l;j++) s+=k[j]*u[l-j];
    float a=rbf(rbf(s)+rbf(u[l]*ds));
    ya[l*D+d]=rbf(bf2f(x[l*D+d])+a);
  }
}
__global__ void g1_k(const u16* xn,const u16* w1,u16* h){
  int row=blockIdx.x; __shared__ float a[D];
  for(int k=threadIdx.x;k<D;k+=blockDim.x) a[k]=bf2f(xn[row*D+k]);
  __syncthreads();
  for(int f=threadIdx.x;f<F;f+=blockDim.x){ float s=0; for(int k=0;k<D;k++) s+=a[k]*bf2f(w1[k*F+f]); s=rbf(s); h[row*F+f]=f2bf(s>0?s:0.f); }
}
__global__ void g2_k(const u16* h,const u16* w2,const float* ya,u16* out){
  int row=blockIdx.x; __shared__ float a[F];
  for(int k=threadIdx.x;k<F;k+=blockDim.x) a[k]=bf2f(h[row*F+k]);
  __syncthreads();
  for(int d=threadIdx.x;d<D;d+=blockDim.x){ float s=0; for(int k=0;k<F;k++) s+=a[k]*bf2f(w2[k*D+d]); out[row*D+d]=f2bf(ya[row*D+d]+rbf(s)); }
}
extern "C" void kernel_launch(void* const* d_in, const int* in_sizes, int n_in, void* d_out, int out_size, void* d_ws, size_t ws_size, hipStream_t stream){
  const u16* x=(const u16*)d_in[0];
  char* ws=(char*)d_ws;
  u16* xn=(u16*)ws; float* K=(float*)(ws+(4<<20)); float* ya=(float*)(ws+(12<<20)); u16* h=(u16*)(ws+(20<<20));
  ln_k<<<L,64,0,stream>>>(x,xn);
  kgen_k<<<D,256,0,stream>>>((const u16*)d_in[1],(const u16*)d_in[2],(const u16*)d_in[3],(const u16*)d_in[4],(const u16*)d_in[5],K);
  conv_k<<<D,256,0,stream>>>(K,xn,x,(const u16*)d_in[6],ya);
  g1_k<<<L,256,0,stream>>>(xn,(const u16*)d_in[8],h);
  g2_k<<<L,256,0,stream>>>(h,(const u16*)d_in[9],ya,(u16*)d_out);
}
```

```cpp
#include <hip/hip_runtime.h>
#include <stdint.h>

typedef unsigned short u16;
typedef unsigned u32;
typedef unsigned long long u64;
typedef u32 v4u __attribute__((ext_vector_type(4)));
typedef u32 v2u __attribute__((ext_vector_type(2)));
typedef u32 v2u_a4 __attribute__((ext_vector_type(2), aligned(4)));
typedef int v4i __attribute__((ext_vector_type(4)));
typedef short v4s __attribute__((ext_vector_type(4)));
typedef float f32x2 __attribute__((ext_vector_type(2)));
typedef float f32x4 __attribute__((ext_vector_type(4)));
typedef float f32x16 __attribute__((ext_vector_type(16)));
typedef __bf16 bf16x2 __attribute__((ext_vector_type(2)));
typedef __bf16 bf16x8 __attribute__((ext_vector_type(8)));
typedef _Float16 f16x2 __attribute__((ext_vector_type(2)));
typedef _Float16 f16x8 __attribute__((ext_vector_type(8)));
typedef short s16x2 __attribute__((ext_vector_type(2)));
#define LAS __attribute__((address_space(3)))
#define GAS __attribute__((address_space(1)))
#define DI __device__ __forceinline__
#define RLX_AGENT __ATOMIC_RELAXED, __HIP_MEMORY_SCOPE_AGENT
#define SB() __builtin_amdgcn_sched_barrier(0)

#define DBG 0

constexpr int L = 2048, D = 512, NS = 128, F = 1792;
constexpr int WS_NONCE = 0;
constexpr int WS_FX = 1024;
constexpr int WS_FH = 2048;
constexpr int WS_FY = 8192;
constexpr int WS_ST = 16384;
constexpr int WS_CS = 32768;
constexpr int WS_XN = 65536;
constexpr int WS_Y = WS_XN + L * D * 2;
constexpr int WS_H = WS_Y + L * D * 2;
constexpr int WS_END = WS_H + L * F * 2;
constexpr int WS_DBG = 16 << 20;
constexpr int HB = 131072;
constexpr int H_U = HB;
constexpr int H_S = H_U + 9216;
constexpr int H_T = H_S + 17408;
constexpr int H_M = H_T + 5312;
constexpr int M_TAG = H_M, M_CNT = H_M + 16, M_RS = H_M + 64;
constexpr int LDS_BYTES = 163840;
constexpr int SPIN_CAP = 1 << 16;
constexpr int AUX_SC1 = 16;

DI float bf_lo(u32 v) { return __uint_as_float(v << 16); }
DI float bf_hi(u32 v) { return __uint_as_float(v & 0xffff0000u); }
DI u32 pk_bf16(float lo, float hi) { f32x2 v = {lo, hi}; return __builtin_bit_cast(u32, __builtin_convertvector(v, bf16x2)); }
DI float rbf(float f) { return bf_lo(pk_bf16(f, f)); }
DI u32 pk_f16(float lo, float hi) { f32x2 v = {lo, hi}; return __builtin_bit_cast(u32, __builtin_convertvector(v, f16x2)); }
DI f32x16 mfma32(v4u a, v4u b, f32x16 c) { return __builtin_amdgcn_mfma_f32_32x32x16_bf16(__builtin_bit_cast(bf16x8, a), __builtin_bit_cast(bf16x8, b), c, 0, 0, 0); }
DI f32x4 mfma16h(v4u a, v4u b, f32x4 c) { return __builtin_amdgcn_mfma_f32_16x16x32_f16(__builtin_bit_cast(f16x8, a), __builtin_bit_cast(f16x8, b), c, 0, 0, 0); }
template <class T> DI T lds_ld(u32 a) { return *(const LAS T*)(uintptr_t)a; }
template <class T> DI void lds_st(u32 a, T v) { *(LAS T*)(uintptr_t)a = v; }
DI v2u lds_tr(u32 a) { return __builtin_bit_cast(v2u, __builtin_amdgcn_ds_read_tr16_b64_v4i16((LAS v4s*)(uintptr_t)a)); }
DI v4u cat(v2u a, v2u b) { v4u r = {a.x, a.y, b.x, b.y}; return r; }
template <int CTRL> DI float dpp_add(float v) { return v + __int_as_float(__builtin_amdgcn_update_dpp(0, __float_as_int(v), CTRL, 0xf, 0xf, true)); }
DI float wave_sum(float v) {
  v = dpp_add<0xB1>(v); v = dpp_add<0x4E>(v); v = dpp_add<0x124>(v); v = dpp_add<0x128>(v);
  float a = __int_as_float(__builtin_amdgcn_readlane(__float_as_int(v), 0)), b = __int_as_float(__builtin_amdgcn_readlane(__float_as_int(v), 16));
  float c = __int_as_float(__builtin_amdgcn_readlane(__float_as_int(v), 32)), d = __int_as_float(__builtin_amdgcn_readlane(__float_as_int(v), 48));
  return (a + b) + (c + d);
}
template <int CTRL> DI u32 dpp_max(u32 v) { u32 o = (u32)__builtin_amdgcn_update_dpp(0, (int)v, CTRL, 0xf, 0xf, true); return v > o ? v : o; }
DI u32 wave_max(u32 v) {
  v = dpp_max<0xB1>(v); v = dpp_max<0x4E>(v); v = dpp_max<0x124>(v); v = dpp_max<0x128>(v);
  u32 a = __builtin_amdgcn_readlane(v, 0), b = __builtin_amdgcn_readlane(v, 16), c = __builtin_amdgcn_readlane(v, 32), d = __builtin_amdgcn_readlane(v, 48);
  a = a > b ? a : b; c = c > d ? c : d; return a > c ? a : c;
}
#define BAR_L() asm volatile("s_waitcnt lgkmcnt(0)\n\ts_barrier" ::: "memory")
#define BAR_V(N) asm volatile("s_waitcnt vmcnt(" #N ") lgkmcnt(0)\n\ts_barrier" ::: "memory")
#define WAIT_V(N) asm volatile("s_waitcnt vmcnt(" #N ")" ::: "memory")
DI void glds(u32 m0, u32 voff, v4i srd, u32 soff) {
  asm volatile("s_mov_b32 m0, %0\n\ts_nop 0\n\tbuffer_load_dwordx4 %1, %2, %3 offen lds" ::"s"(m0), "v"(voff), "s"(srd), "s"(soff) : "memory");
}
DI v4i make_srd(const void* p, u32 bytes) { v4i s; s.x = (int)(uintptr_t)p; s.y = (int)((uintptr_t)p >> 32); s.z = (int)bytes; s.w = 0x00020000; return s; }
DI void grp_sync(int c, u32 n) {
  asm volatile("s_waitcnt lgkmcnt(0)" ::: "memory");
  LAS u32* p = (LAS u32*)(uintptr_t)(M_CNT + 4 * c);
  if ((threadIdx.x & 63) == 0) __hip_atomic_fetch_add(p, 1u, __ATOMIC_RELAXED, __HIP_MEMORY_SCOPE_WORKGROUP);
  for (int i = 0; i < SPIN_CAP * 16; ++i) {
    u32 v = __builtin_amdgcn_readfirstlane(__hip_atomic_load(p, __ATOMIC_RELAXED, __HIP_MEMORY_SCOPE_WORKGROUP));
    if (v >= n) break;
  }
  asm volatile("" ::: "memory");
}
DI void poll_flag(const GAS u32* f, u32 tag) {
  for (int i = 0; i < SPIN_CAP; ++i) {
    u32 v = __hip_atomic_load(f, RLX_AGENT);
    if (__all(v == tag)) break;
    __builtin_amdgcn_s_sleep(1);
  }
  __builtin_amdgcn_fence(__ATOMIC_ACQUIRE, "wavefront");
  asm volatile("" ::: "memory");
}
DI u32 poll_granule(const GAS u64* g, u32 tag) {
  u64 v = 0;
  for (int i = 0; i < SPIN_CAP; ++i) {
    v = __hip_atomic_load(g, RLX_AGENT);
    if (__all((u32)(v >> 32) == tag)) break;
    __builtin_amdgcn_s_sleep(1);
  }
  return (u32)v;
}

struct cpx { float r, i; };
DI cpx cmul(cpx a, cpx b) { cpx c; c.r = __builtin_fmaf(a.r, b.r, -(a.i * b.i)); c.i = __builtin_fmaf(a.r, b.i, a.i * b.r); return c; }
DI cpx zpow(float a2, float th, float tl, float p) {
  float mag = __builtin_amdgcn_exp2f(a2 * p);
  float ph = __builtin_fmaf(tl, p, __builtin_amdgcn_fractf(th * p));
  cpx z; z.r = mag * __builtin_amdgcn_cosf(ph); z.i = mag * __builtin_amdgcn_sinf(ph); return z;
}

#define TS(i) do {} while (0)

__global__ __launch_bounds__(512, 2) void fused_s4d_mlp(const u16* __restrict__ x, const u16* __restrict__ lar, const u16* __restrict__ aim,
                                                        const u16* __restrict__ cre, const u16* __restrict__ cim, const u16* __restrict__ ldt,
                                                        const u16* __restrict__ dsk, const u16* __restrict__ w1, const u16* __restrict__ w2,
                                                        u16* __restrict__ out, char* ws) {
  __shared__ __attribute__((aligned(16))) char smem[LDS_BYTES];
  asm volatile("" ::"v"((u32)(uintptr_t)(LAS char*)smem));
  const int b = blockIdx.x, xj = b & 7, r = b >> 3;
  const int wave = __builtin_amdgcn_readfirstlane(threadIdx.x >> 6);
  const int l = threadIdx.x & 63;
  const int tt = 4 * xj + (r >> 3), dt = r & 7;
  const __amdgpu_buffer_rsrc_t rws = __builtin_amdgcn_make_buffer_rsrc(ws, 0, WS_END, 0x00020000);
  TS(0);

  if (wave < 4) {
    const int hw = wave, rb = 32 * xj + r, c0 = 64 * xj + 2 * r, chl = hw >> 1, hm = hw & 1, ch = c0 + chl;
    const int g = l >> 4, i16 = l & 15;
    const v4u xr0 = *(const GAS v4u*)(x + (size_t)(8 * rb + 2 * hw) * D + 8 * l);
    const v4u xr1 = *(const GAS v4u*)(x + (size_t)(8 * rb + 2 * hw + 1) * D + 8 * l);
    const u32 nonce = __hip_atomic_load((GAS u32*)(ws + WS_NONCE), RLX_AGENT);
    const int pn = ch * NS + 64 * hm + l;
    const u32 p_lar = lar[pn], p_aim = aim[pn], p_cre = cre[pn], p_cim = cim[pn];
    const u32 p_cre2 = cre[pn ^ 64], p_cim2 = cim[pn ^ 64];
    const u32 p_ldt = ldt[ch], p_dsk = dsk[ch];
    {
      const v4u z4 = {0, 0, 0, 0};
      const int t = threadIdx.x;
      if (t < 2) lds_st<v4u>(M_CNT + 16 * t, z4);
    }
    const u32 tag = __builtin_amdgcn_readfirstlane(nonce) * 0x9E3779B1u + 0x7F4A7C15u;
    if (threadIdx.x == 0) lds_st<u32>(M_TAG, tag);
#pragma unroll
    for (int k = 0; k < 2; ++k) {
      const v4u xv = k ? xr1 : xr0;
      float v[8];
#pragma unroll
      for (int j = 0; j < 4; ++j) { v[2 * j] = bf_lo(xv[j]); v[2 * j + 1] = bf_hi(xv[j]); }
      float s = ((v[0] + v[1]) + (v[2] + v[3])) + ((v[4] + v[5]) + (v[6] + v[7]));
      const float mean = wave_sum(s) * (1.0f / D);
      float q = 0.f;
#pragma unroll
      for (int j = 0; j < 8; ++j) { const float d = v[j] - mean; q = __builtin_fmaf(d, d, q); }
      const float var = rbf(wave_sum(q) * (1.0f / D));
      const float m = rbf(mean), sd = rbf(__builtin_sqrtf(rbf(var + 1e-5f)));
      const float rs = 1024.0f / sd;
      v4u o;
#pragma unroll
      for (int j = 0; j < 4; ++j) {
        const u32 d = pk_bf16(v[2 * j] - m, v[2 * j + 1] - m);
        const u32 n = pk_bf16(bf_lo(d) * rs, bf_hi(d) * rs);
        o[j] = pk_f16(bf_lo(n), bf_hi(n));
      }
      lds_st<v4u>(H_U + (2 * hw + k) * 1088 + 16 * l, o);
      if (l == 0) {
        const u32 pay = (__float_as_uint(m) >> 16) | (__float_as_uint(sd) & 0xffff0000u);
        __hip_atomic_store((GAS u64*)(ws + WS_ST) + 8 * rb + 2 * hw + k, ((u64)tag << 32) | pay, RLX_AGENT);
      }
    }
    TS(1);
    BAR_L();
    TS(16);
    {
      const u32 a = H_U + (i16 >> 2) * 1088 + (128 * hw + 16 * g + 4 * (l & 3)) * 2;
#pragma unroll
      for (int i = 0; i < 2; ++i) {
        const v4u o = cat(lds_tr(a + 128 * i), lds_tr(a + 128 * i + 4 * 1088));
        __builtin_amdgcn_raw_buffer_store_b128(o, rws, WS_XN + rb * 8192 + (128 * hw + 64 * i + l) * 16, 0, AUX_SC1);
      }
    }
    const float dtv = rbf(expf(bf_lo(p_ldt)));
    const float alpha = -rbf(expf(bf_lo(p_lar))) * dtv, beta = bf_lo(p_aim) * dtv;
    const float a2 = alpha * 1.44269504088896341f;
    const float th = beta * 0.15915494309189535f;
    const float tl = __builtin_fmaf(beta, 0.15915494309189535f, -th) + beta * 6.42063831672e-09f;
    u32 mx = (p_cre & 0x7fff) > (p_cim & 0x7fff) ? (p_cre & 0x7fff) : (p_cim & 0x7fff);
    const u32 mx2 = (p_cre2 & 0x7fff) > (p_cim2 & 0x7fff) ? (p_cre2 & 0x7fff) : (p_cim2 & 0x7fff);
    mx = wave_max(mx > mx2 ? mx : mx2);
    int ex = (int)(mx >> 7) + 1;
    ex = ex < 32 ? 32 : (ex > 230 ? 230 : ex);
    const float cscale = __uint_as_float((u32)(254 + 14 - ex) << 23);
    const float yscale = __uint_as_float((u32)(ex - 17) << 23) * dtv;
    const cpx z1 = zpow(a2, th, tl, 1.f), z8 = zpow(a2, th, tl, 8.f), z128 = zpow(a2, th, tl, 128.f), z1024 = zpow(a2, th, tl, 1024.f);
    const u32 sw = H_S + hw * 4352 + 4 * l;
    const u32 sr = H_S + hw * 4352 + i16 * 272 + 16 * g;
    BAR_L();
    TS(17);
    v4u yb[4];
    {
      cpx y; y.r = bf_lo(p_cre) * cscale; y.i = bf_lo(p_cim) * cscale;
#pragma unroll
      for (int i = 0; i < 16; ++i) { lds_st<u32>(sw + 272 * i, pk_f16(y.r, -y.i)); if (i < 15) y = cmul(y, z8); }
#pragma unroll
      for (int s = 0; s < 4; ++s) yb[s] = lds_ld<v4u>(sr + 64 * s);
    }
    if (threadIdx.x < 64) { const v4u z4 = {0, 0, 0, 0}; lds_st<v4u>(H_U + (l >> 5) * 4608 + 16 * (l & 31), z4); }
    BAR_V(0);
    TS(18);
    if (threadIdx.x == 0) __hip_atomic_store((GAS u32*)(ws + WS_FX) + rb, tag, RLX_AGENT);
    const u32 tb = H_T + chl * 1344 + 16;
    cpx e0[16];
    f32x4 kacc[3];
    {
      e0[0].r = 32768.f; e0[0].i = 0.f;
      e0[1].r = z1.r * 32768.f; e0[1].i = z1.i * 32768.f;
#pragma unroll
      for (int i = 2; i < 8; ++i) e0[i] = cmul(e0[i - 1], z1);
      e0[8].r = z128.r * 32768.f; e0[8].i = z128.i * 32768.f;
#pragma unroll
      for (int i = 9; i < 16; ++i) e0[i] = cmul(e0[i - 8], z128);
#pragma unroll
      for (int i = 0; i < 16; ++i) lds_st<u32>(sw + 272 * i, pk_f16(e0[i].r, e0[i].i));
      f32x4 acc = {0.f, 0.f, 0.f, 0.f};
#pragma unroll
      for (int s = 0; s < 4; ++s) acc = mfma16h(lds_ld<v4u>(sr + 64 * s), yb[s], acc);
      kacc[0] = acc;
    }
    BAR_L();
    TS(19);
    cpx e1[16];
    {
      const cpx z256 = cmul(z128, z128), z512 = cmul(z256, z256);
#pragma unroll
      for (int i = 0; i < 8; ++i) e1[i] = cmul(e0[8 * (i >> 2) + 2 * (i & 3)], z256);
#pragma unroll
      for (int i = 0; i < 4; ++i) e1[8 + i] = cmul(e0[8 * (i >> 1) + 4 * (i & 1)], z512);
#pragma unroll
      for (int i = 0; i < 4; ++i) e1[12 + i] = cmul(e1[4 * (i >> 1) + 2 * (i & 1)], z512);
#pragma unroll
      for (int i = 0; i < 16; ++i) lds_st<u32>(sw + 272 * i, pk_f16(e1[i].r, e1[i].i));
      f32x4 acc = {0.f, 0.f, 0.f, 0.f};
#pragma unroll
      for (int s = 0; s < 4; ++s) acc = mfma16h(lds_ld<v4u>(sr + 64 * s), yb[s], acc);
      kacc[1] = acc;
    }
    {
      cpx e2[9];
      e2[0] = cmul(e0[0], z1024); e2[1] = cmul(e0[8], z1024); e2[2] = cmul(e1[0], z1024); e2[3] = cmul(e1[4], z1024);
      e2[4] = cmul(e1[8], z1024); e2[5] = cmul(e1[10], z1024); e2[6] = cmul(e1[12], z1024); e2[7] = cmul(e1[14], z1024);
      e2[8] = cmul(e2[0], z1024);
#pragma unroll
      for (int i = 0; i < 9; ++i) lds_st<u32>(sw + 272 * i, pk_f16(e2[i].r, e2[i].i));
      f32x4 acc = {0.f, 0.f, 0.f, 0.f};
#pragma unroll
      for (int s = 0; s < 4; ++s) acc = mfma16h(lds_ld<v4u>(sr + 64 * s), yb[s], acc);
      kacc[2] = acc;
    }
    const u32 kx = H_U + 512 + chl * 4608 + 16 * l;
    if (hm) {
#pragma unroll
      for (int i = 0; i < 3; ++i) lds_st<f32x4>(kx + 1024 * i, kacc[i]);
    }
    TS(2);
    BAR_L();
    TS(20);
    if (!hm) {
      u32 kp[3][2];
#pragma unroll
      for (int i = 0; i < 3; ++i) {
        const f32x4 v = (kacc[i] + lds_ld<f32x4>(kx + 1024 * i)) * 4.76837158203125e-07f;
        kp[i][0] = pk_f16(v[0], v[1]); kp[i][1] = pk_f16(v[2], v[3]);
      }
      lds_st<v2u>(tb + 2 * (128 * (g >> 1) + 4 * (g & 1) + 8 * i16), v2u{kp[0][0], kp[0][1]});
      const u32 a1 = tb + 2 * (g < 2 ? 256 + 64 * g + 4 * i16 : 384 + 64 * (g - 2) + 2 * i16);
      lds_st<u32>(a1, kp[1][0]); lds_st<u32>(a1 + (g < 2 ? 4 : 64), kp[1][1]);
      const u32 a2 = tb + 2 * (512 + 64 * g + i16);
      if (g < 3) lds_st<u16>(a2, (u16)kp[2][0]);
      if (g < 2) { lds_st<u16>(a2 + 32, (u16)(kp[2][0] >> 16)); lds_st<u16>(a2 + 64, (u16)kp[2][1]); lds_st<u16>(a2 + 96, (u16)(kp[2][1] >> 16)); }
      if (l == 63) lds_st<u16>(tb - 2, (u16)0);
    }
    BAR_L();
    TS(21);
    const u32 eb = H_S + chl * 8320;
    {
      const int tau = 64 * hm + l, jtop = 2047 - 16 * tau;
      const float fj = (float)jtop;
      const bool ident = tau >= 112;
      u32 ev[9];
#pragma unroll
      for (int e = 0; e < 18; e += 2) {
        const u32 bits = pk_bf16(fj - (float)e, fj - (float)(e + 1));
        const int i0 = ident ? jtop - e : (int)(bits & 0xffff) - 0x4280;
        const int i1 = ident ? jtop - e - 1 : (int)(bits >> 16) - 0x4280;
        s16x2 v;
        v.x = lds_ld<short>(tb + 2 * i0);
        v.y = e < 16 ? lds_ld<short>(tb + 2 * i1) : (short)0;
        ev[e >> 1] = __builtin_bit_cast(u32, v);
      }
      v4u a0 = {ev[0], ev[1], ev[2], ev[3]}, a1 = {ev[4], ev[5], ev[6], ev[7]}, b0, b1;
#pragma unroll
      for (int e = 0; e < 4; ++e) { b0[e] = __builtin_amdgcn_alignbit(ev[e + 1], ev[e], 16); b1[e] = __builtin_amdgcn_alignbit(ev[e + 5], ev[e + 4], 16); }
      lds_st<v4u>(eb + 32 * tau, a0); lds_st<v4u>(eb + 32 * tau + 16, a1);
      lds_st<v4u>(eb + 4160 + 32 * tau, b0); lds_st<v4u>(eb + 4160 + 32 * tau + 16, b1);
      if (hm == 0 && l < 4) { const v4u z4 = {0, 0, 0, 0}; lds_st<v4u>(eb + 4096 + (l >> 1) * 4160 + 16 * (l & 1), z4); }
    }
    BAR_L();
    TS(22);
    v4u FA[8][4];
    {
      const int x0 = 2047 - 16 * (1 + 8 * hm) - i16 + 8 * g;
      const u32 a = eb + ((i16 & 1) ? 0 : 4160) + 4 * (x0 >> 1) - 512 * 7 - 192;
#pragma unroll
      for (int w = 0; w < 8; w += 2) {
        u32 aw = a + 512 * (6 - w);
        asm volatile("" : "+v"(aw));
#pragma unroll
        for (int w2 = 0; w2 < 2; ++w2)
#pragma unroll
          for (int mi = 0; mi < 4; ++mi) {
            const u32 p = aw + 512 * (1 - w2) + 192 - 64 * mi;
            FA[w + w2][mi] = cat(*(const LAS v2u_a4*)(uintptr_t)p, *(const LAS v2u_a4*)(uintptr_t)(p + 8));
          }
      }
    }
    TS(3);
    BAR_L();
    TS(23);
    BAR_L();
    TS(24);
    poll_flag((const GAS u32*)(ws + WS_FX) + 64 * hw + l, tag);
    TS(4);
    {
      v4u uv[2];
#pragma unroll
      for (int i = 0; i < 2; ++i) uv[i] = __builtin_amdgcn_raw_buffer_load_b128(rws, WS_XN + (64 * hw + 32 * i + (l >> 1)) * 8192 + (c0 + (l & 1)) * 16, 0, AUX_SC1);
#pragma unroll
      for (int i = 0; i < 2; ++i) lds_st<v4u>(H_U + (l & 1) * 4608 + 512 + (64 * hw + 32 * i + (l >> 1)) * 16, uv[i]);
    }
    const v4u xe0 = *(const GAS v4u*)(x + (size_t)(64 * tt + l) * D + 64 * dt + 16 * hw);
    const v4u xe1 = *(const GAS v4u*)(x + (size_t)(64 * tt + l) * D + 64 * dt + 16 * hw + 8);
    grp_sync(0, 4);
    if (b == 0 && threadIdx.x == 0) __hip_atomic_store((GAS u32*)(ws + WS_NONCE), tag, RLX_AGENT);
    TS(5);
    f32x4 acc[8];
#pragma unroll
    for (int i = 0; i < 8; ++i) acc[i] = f32x4{0.f, 0.f, 0.f, 0.f};
    const u32 ub = H_U + chl * 4608 + 512;
    {
      const u32 a = ub + 32 * i16 + 16 * g - 32 * (1 + 8 * hm) - 192;
#pragma unroll
      for (int mi = 0; mi < 4; ++mi) {
        v4u G[8];
#pragma unroll
        for (int v = 0; v < 8; ++v) G[v] = lds_ld<v4u>(a + 512 * v + 192 - 64 * mi);
#pragma unroll
        for (int w = 0; w < 8; ++w)
#pragma unroll
          for (int v = 0; v + w < 8; ++v) acc[v + w] = mfma16h(FA[w][mi], G[v], acc[v + w]);
      }
    }
    TS(6);
    {
      const u32 xw = H_S + hw * 4096 + 16 * l, xrd = H_S + (hw ^ 1) * 4096 + 16 * l;
#pragma unroll
      for (int i = 0; i < 4; ++i) { const f32x4 lo = acc[i], hi = acc[4 + i]; lds_st<f32x4>(xw + 1024 * i, hm ? lo : hi); acc[i] = hm ? hi : lo; }
      grp_sync(1 + chl, 2);
      const float dsv = bf_lo(p_dsk) * (1.0f / 1024.0f);
#pragma unroll
      for (int i = 0; i < 4; ++i) {
        const f32x4 y = (acc[i] + lds_ld<f32x4>(xrd + 1024 * i)) * yscale;
        const int t0 = 256 * (4 * hm + i) + 16 * i16 + 4 * g;
        const v2u uu = lds_ld<v2u>(ub + 2 * t0);
        const u32 ux = uu.x, uy = uu.y;
        const f16x2 u01 = __builtin_bit_cast(f16x2, ux), u23 = __builtin_bit_cast(f16x2, uy);
        const u32 y01 = pk_bf16(y[0], y[1]), y23 = pk_bf16(y[2], y[3]);
        const u32 d01 = pk_bf16((float)u01.x * dsv, (float)u01.y * dsv), d23 = pk_bf16((float)u23.x * dsv, (float)u23.y * dsv);
        v2u o;
        o.x = pk_bf16(bf_lo(y01) + bf_lo(d01), bf_hi(y01) + bf_hi(d01));
        o.y = pk_bf16(bf_lo(y23) + bf_lo(d23), bf_hi(y23) + bf_hi(d23));
        __builtin_amdgcn_raw_buffer_store_b64(o, rws, WS_Y + ch * 4096 + 2 * t0, 0, AUX_SC1);
      }
      WAIT_V(0);
      if (l == 0) __hip_atomic_store((GAS u32*)(ws + WS_FY) + 4 * (c0 >> 1) + hw, tag, RLX_AGENT);
    }
    TS(7);
    grp_sync(3, 4);
    if (hw == 0) {
      const u32 st = poll_granule((const GAS u64*)(ws + WS_ST) + 64 * tt + l, tag);
      lds_st<float>(M_RS + 4 * l, 1.0f / bf_hi(st));
    }
    poll_flag((const GAS u32*)(ws + WS_FY) + 4 * (32 * dt + 8 * hw + ((l & 15) >> 1)) + 2 * (l & 1) + (tt >> 4), tag);
    TS(8);
    {
      const u32 yst = H_S + hw * 4352;
      v4u yv[2];
#pragma unroll
      for (int i = 0; i < 2; ++i) yv[i] = __builtin_amdgcn_raw_buffer_load_b128(rws, WS_Y + (64 * dt + 16 * hw + 8 * i + (l >> 3)) * 4096 + 128 * tt + 16 * (l & 7), 0, AUX_SC1);
#pragma unroll
      for (int i = 0; i < 2; ++i) lds_st<v4u>(yst + (8 * i + (l >> 3)) * 192 + 16 * (l & 7), yv[i]);
      const u32 a = yst + (i16 >> 2) * 192 + 32 * g + 8 * (l & 3);
      const v4u y0 = cat(lds_tr(a), lds_tr(a + 4 * 192)), y1 = cat(lds_tr(a + 8 * 192), lds_tr(a + 12 * 192));
      v4u r0, r1;
#pragma unroll
      for (int j = 0; j < 4; ++j) {
        r0[j] = pk_bf16(bf_lo(xe0[j]) + bf_lo(y0[j]), bf_hi(xe0[j]) + bf_hi(y0[j]));
        r1[j] = pk_bf16(bf_lo(xe1[j]) + bf_lo(y1[j]), bf_hi(xe1[j]) + bf_hi(y1[j]));
      }
      lds_st<v4u>(H_U + l * 144 + 32 * hw, r0);
      lds_st<v4u>(H_U + l * 144 + 32 * hw + 16, r1);
    }
    TS(9);
  } else {
    const int w = wave - 4, wf = w & 1, wt = w >> 1;
    const int h = l >> 5, r32 = l & 31, blk = (l >> 4) & 1, q = (l & 15) >> 2, p = l & 3;
    const v4i sw2 = make_srd(w2, F * D * 2);
    const u32 v_w2 = (l >> 3) * 1024 + (((l & 7) ^ (((l >> 4) & 1) << 2)) * 16);
    const u32 s_w2 = (448 * w) * 1024 + 128 * dt;
#define W2_PIECE1(P, SLOT, I) glds((SLOT) * 32768 + w * 8192 + (I) * 1024, v_w2, sw2, s_w2 + ((P) * 64 + (I) * 8) * 1024)
#define W2_PIECE(P, SLOT) do { _Pragma("unroll") for (int i_ = 0; i_ < 8; ++i_) W2_PIECE1(P, SLOT, i_); } while (0)
    u32 tag;
    if (r < 28) {
      const int mt = 4 * (xj >> 1) + r / 7, nt = 7 * (xj & 1) + r % 7;
      const v4i sx = make_srd(x, L * D * 2), sw1 = make_srd(w1, D * F * 2);
      const u32 v_x0 = (l >> 3) * 1024 + (((l & 7) ^ (l >> 4)) * 16), v_x1 = v_x0 ^ 64;
      const u32 v_w1 = (l >> 4) * 3584 + (((l & 15) ^ ((l >> 4) << 2)) * 16);
      const u32 s_x = (128 * mt + 32 * w) * 1024, s_w1 = (16 * w) * 3584 + 256 * nt;
#define G1_PIECE1(T, I) do { if ((I) < 4) glds(((T) & 3) * 32768 + (4 * w + (I)) * 1024, ((I) & 1) ? v_x1 : v_x0, sx, s_x + (I) * 8192 + (T) * 128); \
                             else glds(((T) & 3) * 32768 + 16384 + (4 * w + (I) - 4) * 1024, v_w1, sw1, s_w1 + ((T) * 64 + ((I) - 4) * 4) * 3584); } while (0)
#define NEXT1(T, I) do { if ((T) < 8) G1_PIECE1(T, I); else W2_PIECE1((T) - 8, (T) & 3, I); } while (0)
#pragma unroll
      for (int i = 0; i < 8; ++i) G1_PIECE1(0, i);
#pragma unroll
      for (int i = 0; i < 8; ++i) G1_PIECE1(1, i);
      u32 ab[4], aa[2];
      {
        const int row = 64 * wt + r32, z = (row >> 1) & 7;
#pragma unroll
        for (int s = 0; s < 4; ++s) ab[s] = row * 128 + (((2 * s + h) ^ z) * 16);
#pragma unroll
        for (int f2 = 0; f2 < 2; ++f2) aa[f2] = 16384 + (8 * h + q) * 256 + (((8 * wf + 4 * f2 + 2 * blk + (p >> 1)) ^ (q << 2)) * 16) + 8 * (p & 1);
      }
      f32x16 acc[2][2];
#pragma unroll
      for (int i = 0; i < 2; ++i)
#pragma unroll
        for (int j = 0; j < 2; ++j)
#pragma unroll
          for (int e = 0; e < 16; ++e) acc[i][j][e] = 0.f;
      v4u fa[2][2], fb[2][2];
      v2u gm[2], gc[2];
#define G1_FRAGS(K) do { const u32 sl_ = (((K) >> 2) & 3) * 32768; \
        fb[(K) & 1][0] = lds_ld<v4u>(ab[(K) & 3] + sl_); fb[(K) & 1][1] = lds_ld<v4u>(ab[(K) & 3] + sl_ + 4096); \
        fa[(K) & 1][0] = cat(lds_tr(aa[0] + sl_ + ((K) & 3) * 4096), lds_tr(aa[0] + sl_ + ((K) & 3) * 4096 + 1024)); \
        fa[(K) & 1][1] = cat(lds_tr(aa[1] + sl_ + ((K) & 3) * 4096), lds_tr(aa[1] + sl_ + ((K) & 3) * 4096 + 1024)); } while (0)
      BAR_V(8);
      TS(1);
      G1_FRAGS(0);
      NEXT1(2, 0); NEXT1(2, 1);
      const v4i sws = make_srd(ws, WS_END);
      const u32 v_g = r32 * 8;
#pragma unroll
      for (int k = 0; k < 32; ++k) {
        const int t = k >> 2, s = k & 3;
        if (s == 3) {
          if (t == 5) BAR_V(12); else if (t < 7) BAR_V(8); else BAR_L();
          if (t == 6) asm volatile("" : "+v"(gm[0]), "+v"(gm[1]), "+v"(gc[0]), "+v"(gc[1]));
          TS(17 + t);
        }
        if (k < 31) G1_FRAGS(k + 1);
        SB();
        const int tn = (s == 3) ? t + 3 : t + 2, in = (s == 3) ? 0 : 2 * s + 2;
        acc[0][0] = mfma32(fa[k & 1][0], fb[k & 1][0], acc[0][0]);
        acc[0][1] = mfma32(fa[k & 1][0], fb[k & 1][1], acc[0][1]);
        SB();
        NEXT1(tn, in);
        SB();
        acc[1][0] = mfma32(fa[k & 1][1], fb[k & 1][0], acc[1][0]);
        acc[1][1] = mfma32(fa[k & 1][1], fb[k & 1][1], acc[1][1]);
        SB();
        NEXT1(tn, in + 1);
        if (k == 22) {
          const u32 so_m = WS_ST + (128 * mt + 64 * wt) * 8, so_c = WS_CS + (128 * nt + 64 * wf) * 8;
          asm volatile("buffer_load_dwordx2 %0, %4, %5, %6 offen sc1\n\tbuffer_load_dwordx2 %1, %4, %5, %6 offen offset:256 sc1\n\t"
                       "buffer_load_dwordx2 %2, %4, %5, %7 offen sc1\n\tbuffer_load_dwordx2 %3, %4, %5, %7 offen offset:256 sc1"
                       : "=&v"(gm[0]), "=&v"(gm[1]), "=&v"(gc[0]), "=&v"(gc[1]) : "v"(v_g), "s"(sws), "s"(so_m), "s"(so_c) : "memory");
        }
        SB();
      }
      TS(2);
      tag = __builtin_amdgcn_readfirstlane(lds_ld<u32>(M_TAG));
      if (!__all(gm[0].y == tag && gm[1].y == tag && gc[0].y == tag && gc[1].y == tag)) {
#pragma unroll
        for (int i = 0; i < 2; ++i) {
          gm[i].x = poll_granule((const GAS u64*)(ws + WS_ST) + 128 * mt + 64 * wt + 32 * i + r32, tag);
          gc[i].x = poll_granule((const GAS u64*)(ws + WS_CS) + 128 * nt + 64 * wf + 32 * i + r32, tag);
        }
      }
      {
        v4u ca[2], cb[2];
#pragma unroll
        for (int i = 0; i < 2; ++i) {
          ca[i] = v4u{h ? 0u : gc[i].x, 0u, 0u, 0u};
          const u32 nm = (gm[i].x & 0xffffu) ^ 0x8000u;
          cb[i] = v4u{nm | (nm << 16), 0u, 0u, 0u};
        }
#pragma unroll
        for (int i = 0; i < 2; ++i)
#pragma unroll
          for (int j = 0; j < 2; ++j) acc[i][j] = mfma32(ca[i], cb[j], acc[i][j]);
      }
#pragma unroll
      for (int i = 0; i < 2; ++i)
#pragma unroll
        for (int j = 0; j < 2; ++j)
#pragma unroll
          for (int s = 0; s < 2; ++s) {
            v4u o;
#pragma unroll
            for (int e = 0; e < 4; ++e) {
              const s16x2 v = __builtin_bit_cast(s16x2, pk_bf16(acc[i][j][8 * s + 2 * e], acc[i][j][8 * s + 2 * e + 1]));
              const s16x2 z = {0, 0};
              o[e] = __builtin_bit_cast(u32, __builtin_elementwise_max(v, z));
            }
            __builtin_amdgcn_raw_buffer_store_b128(o, rws, 16 * l, WS_H + ((4 * mt + 2 * wt + j) * 112 + 8 * nt + 4 * wf + 2 * i + s) * 1024, AUX_SC1);
          }
      SB();
#pragma unroll
      for (int i = 2; i < 8; ++i) W2_PIECE1(2, 2, i);
      W2_PIECE(3, 3);
      TS(3);
      WAIT_V(14);
      if (l == 0) __hip_atomic_store((GAS u32*)(ws + WS_FH) + (2 * mt + wt) * 32 + 2 * nt + wf, tag, RLX_AGENT);
      TS(4);
    } else {
      const int ii = 4 * xj + r - 28;
      float cs[8];
#pragma unroll
      for (int j = 0; j < 8; ++j) cs[j] = 0.f;
      v4u wv[16];
      const int cb = ii < 28 ? ii : 0;
#pragma unroll
      for (int i = 0; i < 16; ++i) wv[i] = *(const GAS v4u*)(w1 + (size_t)(128 * w + 8 * i + (l >> 3)) * F + 64 * cb + 8 * (l & 7));
      SB();
      BAR_L();
      BAR_L();
      SB();
#pragma unroll
      for (int i = 0; i < 16; ++i)
#pragma unroll
        for (int j = 0; j < 4; ++j) { cs[2 * j] += bf_lo(wv[i][j]); cs[2 * j + 1] += bf_hi(wv[i][j]); }
      lds_st<f32x4>(98304 + (8 * w + (l >> 3)) * 272 + 32 * (l & 7), f32x4{cs[0], cs[1], cs[2], cs[3]});
      lds_st<f32x4>(98304 + (8 * w + (l >> 3)) * 272 + 32 * (l & 7) + 16, f32x4{cs[4], cs[5], cs[6], cs[7]});
      W2_PIECE(0, 0); W2_PIECE(1, 1); W2_PIECE(2, 2);
      BAR_L();
      tag = __builtin_amdgcn_readfirstlane(lds_ld<u32>(M_TAG));
      if (ii < 28 && w == 0) {
        float s = 0.f;
#pragma unroll
        for (int i = 0; i < 32; ++i) s += lds_ld<float>(98304 + i * 272 + 4 * l);
        const float hi = rbf(s);
        const u32 pay = pk_bf16(hi, s - hi);
        __hip_atomic_store((GAS u64*)(ws + WS_CS) + 64 * ii + l, ((u64)tag << 32) | pay, RLX_AGENT);
      }
      BAR_L();
      W2_PIECE(3, 3);
      BAR_L(); BAR_L(); BAR_L(); BAR_L(); BAR_L();
      WAIT_V(8);
    }
    u32 a2[2];
#pragma unroll
    for (int d2 = 0; d2 < 2; ++d2) a2[d2] = w * 8192 + (4 * h + q) * 128 + (((4 * d2 + 2 * blk + (p >> 1)) ^ (((q >> 1) & 1) << 2)) * 16) + 8 * (p & 1);
#define W2_FRAG(SLOT, S, D2) cat(lds_tr(a2[D2] + (SLOT) * 32768 + (S) * 2048), lds_tr(a2[D2] + (SLOT) * 32768 + (S) * 2048 + 1024))
    v4u pa[12][2];
#define PARK(P) do { _Pragma("unroll") for (int s_ = 0; s_ < 4; ++s_) { pa[4 * (P) + s_][0] = W2_FRAG(P, s_, 0); pa[4 * (P) + s_][1] = W2_FRAG(P, s_, 1); } \
                     asm volatile("s_waitcnt lgkmcnt(0)" ::: "memory"); } while (0)
    PARK(0); W2_PIECE(4, 0);
    PARK(1); W2_PIECE(5, 1);
    WAIT_V(24);
    PARK(2); W2_PIECE(6, 2);
    TS(5);
    poll_flag((const GAS u32*)(ws + WS_FH) + tt * 32 + 7 * w + (l < 7 ? l : 0), tag);
    WAIT_V(0);
    TS(6);
    f32x16 acc[2][2];
#pragma unroll
    for (int i = 0; i < 2; ++i)
#pragma unroll
      for (int j = 0; j < 2; ++j)
#pragma unroll
        for (int e = 0; e < 16; ++e) acc[i][j][e] = 0.f;
    constexpr int WIN = 6;
    v4u hb[WIN][2];
    const u32 s_h = WS_H + ((2 * tt) * 112 + 28 * w) * 1024;
#define H_LOAD(K, T2) __builtin_amdgcn_raw_buffer_load_b128(rws, 16 * l, s_h + ((T2) * 112 + (K)) * 1024, AUX_SC1)
#pragma unroll
    for (int k = 0; k < WIN; ++k) { hb[k][0] = H_LOAD(k, 0); hb[k][1] = H_LOAD(k, 1); }
    SB();
    v4u fa[2][2];
#pragma unroll
    for (int k = 0; k < 28; ++k) {
      if (k + 1 >= 12 && k + 1 < 28) { const int pc = (k + 1) >> 2, sl = pc & 3; fa[(k + 1) & 1][0] = W2_FRAG(sl, (k + 1) & 3, 0); fa[(k + 1) & 1][1] = W2_FRAG(sl, (k + 1) & 3, 1); }
      SB();
      const v4u a0 = k < 12 ? pa[k < 12 ? k : 0][0] : fa[k & 1][0], a1 = k < 12 ? pa[k < 12 ? k : 0][1] : fa[k & 1][1];
      const v4u b0 = hb[k % WIN][0], b1 = hb[k % WIN][1];
      acc[0][0] = mfma32(a0, b0, acc[0][0]);
      acc[1][0] = mfma32(a1, b0, acc[1][0]);
      SB();
      if (k + WIN < 28) hb[k % WIN][0] = H_LOAD(k + WIN, 0);
      SB();
      acc[0][1] = mfma32(a0, b1, acc[0][1]);
      acc[1][1] = mfma32(a1, b1, acc[1][1]);
      SB();
      if (k + WIN < 28) hb[k % WIN][1] = H_LOAD(k + WIN, 1);
      SB();
    }
    TS(7);
#pragma unroll
    for (int j = 0; j < 2; ++j)
#pragma unroll
      for (int i = 0; i < 2; ++i)
#pragma unroll
        for (int e = 0; e < 4; ++e) {
          const f32x4 v = {acc[i][j][4 * e], acc[i][j][4 * e + 1], acc[i][j][4 * e + 2], acc[i][j][4 * e + 3]};
          lds_st<f32x4>(j * 32768 + w * 8192 + r32 * 256 + (((8 * i + 2 * e + h) ^ (r32 & 7)) * 16), v);
        }
    TS(8);
  }
  BAR_L();
  {
    const int t = threadIdx.x, tok = t >> 3, o = t & 7;
    const u32 a = (tok >> 5) * 32768 + (tok & 31) * 256;
    const u32 c0a = a + (((2 * o) ^ (tok & 7)) * 16), c1a = a + (((2 * o + 1) ^ (tok & 7)) * 16);
    f32x4 s0 = (lds_ld<f32x4>(c0a) + lds_ld<f32x4>(c0a + 8192)) + (lds_ld<f32x4>(c0a + 16384) + lds_ld<f32x4>(c0a + 24576));
    f32x4 s1 = (lds_ld<f32x4>(c1a) + lds_ld<f32x4>(c1a + 8192)) + (lds_ld<f32x4>(c1a + 16384) + lds_ld<f32x4>(c1a + 24576));
    const float rs = lds_ld<float>(M_RS + 4 * tok);
    const v4u R = lds_ld<v4u>(H_U + tok * 144 + 16 * o);
    s0 *= rs; s1 *= rs;
    const u32 b0 = pk_bf16(s0[0], s0[1]), b1 = pk_bf16(s0[2], s0[3]), b2 = pk_bf16(s1[0], s1[1]), b3 = pk_bf16(s1[2], s1[3]);
    v4u res;
    res.x = pk_bf16(bf_lo(R.x) + bf_lo(b0), bf_hi(R.x) + bf_hi(b0));
    res.y = pk_bf16(bf_lo(R.y) + bf_lo(b1), bf_hi(R.y) + bf_hi(b1));
    res.z = pk_bf16(bf_lo(R.z) + bf_lo(b2), bf_hi(R.z) + bf_hi(b2));
    res.w = pk_bf16(bf_lo(R.w) + bf_lo(b3), bf_hi(R.w) + bf_hi(b3));
    *(GAS v4u*)(out + (size_t)(64 * tt + tok) * D + 64 * dt + 8 * o) = res;
  }
}

extern "C" void kernel_launch(void* const* d_in, const int* in_sizes, int n_in, void* d_out, int out_size, void* d_ws, size_t ws_size,
                              hipStream_t stream) {
  fused_s4d_mlp<<<256, 512, 0, stream>>>((const u16*)d_in[0], (const u16*)d_in[1], (const u16*)d_in[2], (const u16*)d_in[3], (const u16*)d_in[4],
                                         (const u16*)d_in[5], (const u16*)d_in[6], (const u16*)d_in[8], (const u16*)d_in[9], (u16*)d_out,
                                         (char*)d_ws);
}
```

```cpp
#include <hip/hip_runtime.h>
#include <stdint.h>

typedef unsigned short u16;
typedef unsigned u32;
typedef unsigned long long u64;
typedef u32 v4u __attribute__((ext_vector_type(4)));
typedef u32 v2u __attribute__((ext_vector_type(2)));
typedef u32 v2u_a4 __attribute__((ext_vector_type(2), aligned(4)));
typedef int v4i __attribute__((ext_vector_type(4)));
typedef int v2i __attribute__((ext_vector_type(2)));
typedef int v16i __attribute__((ext_vector_type(16)));
typedef short v4s __attribute__((ext_vector_type(4)));
typedef float f32x2 __attribute__((ext_vector_type(2)));
typedef float f32x4 __attribute__((ext_vector_type(4)));
typedef float f32x16 __attribute__((ext_vector_type(16)));
typedef __bf16 bf16x2 __attribute__((ext_vector_type(2)));
typedef __bf16 bf16x8 __attribute__((ext_vector_type(8)));
typedef _Float16 f16x2 __attribute__((ext_vector_type(2)));
typedef _Float16 f16x8 __attribute__((ext_vector_type(8)));
typedef short s16x2 __attribute__((ext_vector_type(2)));
#define LAS __attribute__((address_space(3)))
#define GAS __attribute__((address_space(1)))
#if defined(__HIP_DEVICE_COMPILE__)
#define NOPK __attribute__((target("no-packed-fp32-ops")))
#else
#define NOPK
#endif
#define DI NOPK __device__ __forceinline__
#define RLX_AGENT __ATOMIC_RELAXED, __HIP_MEMORY_SCOPE_AGENT
#define SB() __builtin_amdgcn_sched_barrier(0)
#define PIN(x) asm volatile("" : "+v"(x))
#define UNROLL _Pragma("unroll")

#define DBG 0
#define NPRE 8
#define NM1 8
#define NM2 4
#define NPRO 20
#define KGRAN 22
#define SLP 1
#define PRIO 2
#define PSLP 2
#define NRL 4
#define NB 8
#define NFILL 8
#define YB0 4
#define YB1 12
#define HPRIO 3
#define EARLY 1
#define HWIN 5
#define XSLOT 1

#define ND0 ((32 - NPRO) / 4)
#define IS_D(K) ((K) >= 3 && (K) <= 18)
#define MIN_(A, B) ((A) < (B) ? (A) : (B))
#define RAW_BEFORE(K) (2 * ((K) - MIN_(K, ND0) - ((K) < 3 ? 0 : (K) > 19 ? 16 : (K) - 3)))
#define BEFORE(K) (NPRO + 2 * (K) + 2 * MIN_(K, ND0) + ((K) > KGRAN ? 4 : 0))
#define LASTPOS(T) ((T) <= 3 ? 8 * (T) + 7 : BEFORE(4 * (T) - 10) + 1)
#define GPOS (BEFORE(KGRAN + 1) - 1)
static_assert((NPRO & 3) == 0 && NPRO >= 20 && NPRO <= 32 && KGRAN >= 18 && KGRAN < 27, "load schedule");

constexpr int L = 2048, D = 512, NS = 128, F = 1792;
constexpr int WS_NONCE = 0;
constexpr int FL = 32;
constexpr int WS_CS = 16384;
constexpr int WS_ST = 32768;
constexpr int WS_FH = 65536;
constexpr int WS_FY = WS_FH + 131072;
constexpr int WS_XN = 524288;
constexpr int WS_Y = WS_XN + L * D * 2;
constexpr int WS_H = WS_Y + L * D * 2;
constexpr int WS_END = WS_H + L * F * 2;
constexpr int WS_DBG = 16 << 20;
constexpr int HB = 131072;
constexpr int H_U = HB;
constexpr int H_S = H_U + 9216;
constexpr int H_T = H_S + 17408;
constexpr int M_CF = H_T + 2688;
constexpr int M_HF = M_CF + 1024;
constexpr int M_TAG = M_HF + 1024;
constexpr int M_RS = M_TAG + 16;
constexpr int M_ST = M_RS + 256;
constexpr int LDS_BYTES = 163840;
constexpr int SPIN_CAP = 1 << 16;
constexpr int AUX_SC1 = 16;

DI float asf(u32 v) { return __builtin_bit_cast(float, v); }
DI u32 asu(float v) { return __builtin_bit_cast(u32, v); }
DI float bf_lo(u32 v) { return asf(v << 16); }
DI float bf_hi(u32 v) { return asf(v & 0xffff0000u); }
DI u32 pk_bf16(float lo, float hi) { f32x2 v = {lo, hi}; return __builtin_bit_cast(u32, __builtin_convertvector(v, bf16x2)); }
DI float rbf(float f) { return bf_lo(pk_bf16(f, f)); }
DI u32 pk_f16(float lo, float hi) { f32x2 v = {lo, hi}; return __builtin_bit_cast(u32, __builtin_convertvector(v, f16x2)); }
DI f32x16 mfma32(v4u a, v4u b, f32x16 c) { return __builtin_amdgcn_mfma_f32_32x32x16_bf16(__builtin_bit_cast(bf16x8, a), __builtin_bit_cast(bf16x8, b), c, 0, 0, 0); }
DI f32x4 mfma16h(v4u a, v4u b, f32x4 c) { return __builtin_amdgcn_mfma_f32_16x16x32_f16(__builtin_bit_cast(f16x8, a), __builtin_bit_cast(f16x8, b), c, 0, 0, 0); }
template <class T> DI T lds_ld(u32 a) { return *(const LAS T*)(uintptr_t)a; }
template <class T> DI void lds_st(u32 a, T v) { *(LAS T*)(uintptr_t)a = v; }
DI v2u lds_tr(u32 a) { return __builtin_bit_cast(v2u, __builtin_amdgcn_ds_read_tr16_b64_v4i16((LAS v4s*)(uintptr_t)a)); }
DI v4u cat(v2u a, v2u b) { v4u r = {a.x, a.y, b.x, b.y}; return r; }
DI bool all64(bool c) { return __builtin_amdgcn_ballot_w64(c) == ~0ull; }
DI u32 umin(u32 a, u32 b) { return a < b ? a : b; }
DI u32 umax(u32 a, u32 b) { return a > b ? a : b; }
template <int CTRL, int RM> DI float dpp_add(float v) { return v + asf(__builtin_amdgcn_update_dpp(0, asu(v), CTRL, RM, 0xf, true)); }
template <int CTRL, int RM> DI u32 dpp_max(u32 v) { return umax(v, (u32)__builtin_amdgcn_update_dpp(0, (int)v, CTRL, RM, 0xf, true)); }
#define BAR_L() asm volatile("s_waitcnt lgkmcnt(0)\n\ts_barrier" ::: "memory")
#define BAR_V(N) asm volatile("s_waitcnt vmcnt(%0) lgkmcnt(0)\n\ts_barrier" ::"n"(N) : "memory")
#define HBAR() do { SB(); BAR_L(); SB(); } while (0)
#define NBAR() do { SB(); asm volatile("s_barrier" ::: "memory"); SB(); } while (0)
#define WAIT_V(N) asm volatile("s_waitcnt vmcnt(%0)" ::"n"(N) : "memory")
#define FENCE() asm volatile("" ::: "memory")
DI v4i make_srd(const void* p, u32 bytes) { v4i s; s.x = (int)(uintptr_t)p; s.y = (int)((uintptr_t)p >> 32); s.z = (int)bytes; s.w = 0x00020000; return s; }
DI void rec_post(u32 rec, int w, u32 v) { FENCE(); *(volatile LAS u32*)(uintptr_t)(rec + 4 * w) = v; FENCE(); }
DI void rec_wait4(u32 rec, u32 v) {
#pragma unroll 1
  for (int i = 0; i < SPIN_CAP * 16; ++i) {
    const v4u f = *(volatile LAS v4u*)(uintptr_t)rec;
    if (all64(umin(umin(f.x, f.y), umin(f.z, f.w)) >= v)) break;
    __builtin_amdgcn_s_sleep(SLP);
  }
  FENCE();
}
DI void rec_wait1(u32 rec, int w, u32 v) {
#pragma unroll 1
  for (int i = 0; i < SPIN_CAP * 16; ++i) {
    const u32 f = *(volatile LAS u32*)(uintptr_t)(rec + 4 * w);
    if (all64(f >= v)) break;
    __builtin_amdgcn_s_sleep(SLP);
  }
  FENCE();
}
DI void poll_flag(const GAS u32* f, u32 tag) {
#pragma unroll 1
  for (int i = 0; i < SPIN_CAP; ++i) {
    const u32 v = __hip_atomic_load(f, RLX_AGENT);
    if (all64(v == tag)) break;
    __builtin_amdgcn_s_sleep(PSLP);
  }
}
#define AFTER_POLL() do { __builtin_amdgcn_fence(__ATOMIC_ACQUIRE, "wavefront"); FENCE(); } while (0)
DI u32 poll_granule(const GAS u64* g, u32 tag) {
  u64 v = 0;
#pragma unroll 1
  for (int i = 0; i < SPIN_CAP; ++i) {
    v = __hip_atomic_load(g, RLX_AGENT);
    if (all64((u32)(v >> 32) == tag)) break;
    __builtin_amdgcn_s_sleep(1);
  }
  return (u32)v;
}
DI float dot2(u32 a, u32 b, float c) { return __builtin_amdgcn_fdot2_f32_bf16(__builtin_bit_cast(bf16x2, a), __builtin_bit_cast(bf16x2, b), c, false); }
template <int V> struct ic { static constexpr int v = V; };
template <int I, int N, class Fn> DI void static_for(Fn&& f) { if constexpr (I < N) { f(ic<I>{}); static_for<I + 1, N>(f); } }
#define SFOR(VAR, N0, N1) static_for<N0, N1>([&](auto VAR##_) NOPK __attribute__((always_inline)) { constexpr int VAR = decltype(VAR##_)::v;
#define SEND });
struct cpx { float r, i; };
DI cpx cmul(cpx a, cpx b) { cpx c; c.r = __builtin_fmaf(a.r, b.r, -(a.i * b.i)); c.i = __builtin_fmaf(a.r, b.i, a.i * b.r); return c; }
DI cpx zpow(float a2, float th, float tl, float p) {
  const float mag = __builtin_amdgcn_exp2f(a2 * p);
  const float ph = __builtin_fmaf(tl, p, __builtin_amdgcn_fractf(th * p));
  cpx z; z.r = mag * __builtin_amdgcn_cosf(ph); z.i = mag * __builtin_amdgcn_sinf(ph); return z;
}
DI float exp_f(float x) {
  const float t = x * 1.44269502162933349609375f;
  const float e = __builtin_fmaf(x, 1.925963033500011079e-08f, __builtin_fmaf(x, 1.44269502162933349609375f, -t));
  const float r = __builtin_amdgcn_exp2f(t);
  const float c = __builtin_fmaf(r * e, 0.693147180559945f, r);
  return c == c ? c : r;
}

#define TS(i) do {} while (0)

#define HTS(i) do {} while (0)
#define CTS 0
NOPK __global__ __launch_bounds__(512, 2) void fused_s4d_mlp(const u16* a_x, const u16* a_lar, const u16* a_aim, const u16* a_cre, const u16* a_cim,
                                                             const u16* a_ldt, const u16* a_dsk, const u16* a_w1, const u16* a_w2, u16* a_out, char* a_ws) {
  __shared__ __attribute__((aligned(16))) char smem[LDS_BYTES];
  asm volatile("" ::"v"((u32)(uintptr_t)(LAS char*)smem));
  typedef __attribute__((address_space(4))) const char* kptr;
  const kptr kas = (kptr)__builtin_amdgcn_kernarg_segment_ptr();
  const v16i ka = *(volatile __attribute__((address_space(4))) const v16i*)kas;
  const v4i kb = *(volatile __attribute__((address_space(4))) const v4i*)(kas + 64);
  const v2i kc = *(volatile __attribute__((address_space(4))) const v2i*)(kas + 80);
  SB();
#define ARGS_WAIT() SB()
#define PTR(V, I) ((char*)(((u64)(u32)(V)[2 * (I) + 1] << 32) | (u32)(V)[2 * (I)]))
#define RSRC(P, BYTES) __builtin_amdgcn_make_buffer_rsrc(P, 0, BYTES, 0x00020000)
  const int b = __builtin_amdgcn_workgroup_id_x(), xj = b & 7, r = b >> 3;
  const int tid = __builtin_amdgcn_workitem_id_x();
  const int wave = __builtin_amdgcn_readfirstlane(tid >> 6);
  const int l = tid & 63;
  const int tt = 4 * xj + (r >> 3), dt = r & 7;
  const v4u z4 = {0, 0, 0, 0};
  const u32 ooff = (64 * tt + (tid >> 3)) * 1024 + 128 * dt + 16 * (tid & 7);
  v4u xo;

  if (wave < 4) {
    const int hw = wave, rb = 32 * xj + r, c0 = 64 * xj + 2 * r, chl = hw >> 1, hm = hw & 1, ch = c0 + chl;
    const int g = l >> 4, i16 = l & 15;
    float p_lar = 0.f, p_aim = 0.f, p_cre = 0.f, p_cim = 0.f, p_cre2 = 0.f, p_cim2 = 0.f, p_ldt = 0.f, p_dsk = 0.f;
    u32 nonce;
    {
      u32 v1 = (ch * NS + 64 * hm + l) * 2, v2 = v1 ^ 128, v3 = ch * 2, v4 = 0;
      PIN(v1); PIN(v2); PIN(v3); PIN(v4);
      ARGS_WAIT();
      asm volatile("global_load_short_d16_hi %0, %9, %13\n\tglobal_load_short_d16_hi %6, %11, %17\n\tglobal_load_short_d16_hi %1, %9, %14\n\t"
                   "global_load_short_d16_hi %2, %9, %15\n\tglobal_load_short_d16_hi %3, %9, %16\n\tglobal_load_short_d16_hi %4, %10, %15\n\t"
                   "global_load_short_d16_hi %5, %10, %16\n\tglobal_load_short_d16_hi %7, %11, %18\n\tglobal_load_dword %8, %12, %19 sc1"
                   : "+&v"(p_lar), "+&v"(p_aim), "+&v"(p_cre), "+&v"(p_cim), "+&v"(p_cre2), "+&v"(p_cim2), "+&v"(p_ldt), "+&v"(p_dsk), "=&v"(nonce)
                   : "v"(v1), "v"(v2), "v"(v3), "v"(v4), "s"(PTR(ka, 1)), "s"(PTR(ka, 2)), "s"(PTR(ka, 3)), "s"(PTR(ka, 4)), "s"(PTR(ka, 5)),
                     "s"(PTR(ka, 6)), "s"(PTR(kc, 0)) : "memory");
    }
    char* const ws = PTR(kc, 0);
    const __amdgpu_buffer_rsrc_t rws = RSRC(ws, WS_END);
    TS(12);
    if (hw >= 2) lds_st<v4u>(M_CF + 16 * (tid - 128), z4);
    const u32 hrec = M_HF + 16 * l;
    const u32 tb = H_T + chl * 1344 + 16;
    u32 xa[17];
    {
      const int tau = 64 * hm + l, jtop = 2047 - 16 * tau;
      float fj = (float)jtop;
      PIN(fj);
      const bool ident = tau >= 112;
      const u32 sel = ident ? 0u : 0x1fffeu, base = ident ? tb + 2 * jtop : tb - 2 * 0x4280, stp = ident ? 2u : 0u;
      UNROLL for (int e = 0; e < 18; e += 2) {
        const u32 bits = pk_bf16(fj - (float)e, fj - (float)(e + 1));
        xa[e] = ((bits << 1) & sel) + (base - e * stp);
        if (e < 16) xa[e + 1] = ((bits >> 15) & sel) + (base - (e + 1) * stp);
      }
      UNROLL for (int e = 0; e < 17; ++e) PIN(xa[e]);
    }
    u32 sw[4];
    UNROLL for (int i = 0; i < 4; ++i) { sw[i] = H_S + hw * 4352 + 4 * l + 1088 * i; PIN(sw[i]); }
    u32 sr = H_S + hw * 4352 + i16 * 272 + 16 * g;
    u32 lnr = H_U + (i16 >> 2) * 1088 + (128 * hw + 16 * g + 4 * (l & 3)) * 2;
    u32 xno = WS_XN + rb * 8192 + (128 * hw + l) * 16;
    u32 uoff = WS_XN + (64 * hw + (l >> 1)) * 8192 + (c0 + (l & 1)) * 16;
    u32 ta0 = tb + 2 * (128 * (g >> 1) + 4 * (g & 1) + 8 * i16), ta1 = tb + 2 * (g < 2 ? 256 + 64 * g + 4 * i16 : 384 + 64 * (g - 2) + 2 * i16);
    u32 ta1b = ta1 + (g < 2 ? 4 : 64), ta2 = g < 3 ? tb + 2 * (512 + 64 * g + i16) : tb - 2;
    PIN(sr); PIN(lnr); PIN(xno); PIN(uoff); PIN(ta0); PIN(ta1); PIN(ta1b); PIN(ta2);
    TS(10);
    asm volatile("s_waitcnt vmcnt(0)" : "+v"(p_lar), "+v"(p_aim), "+v"(p_cre), "+v"(p_cim), "+v"(p_cre2), "+v"(p_cim2), "+v"(p_ldt), "+v"(p_dsk), "+v"(nonce)::"memory");
    TS(11);
    const u32 tag = __builtin_amdgcn_readfirstlane(nonce) * 0x9E3779B1u + 0x7F4A7C15u;
    if (tid == 0) lds_st<u32>(M_TAG, tag);
    const float dtv = rbf(exp_f(p_ldt));
    const float alpha = -rbf(exp_f(p_lar)) * dtv, beta = p_aim * dtv;
    const float a2 = alpha * 1.44269504088896341f;
    const float th = beta * 0.15915494309189535f;
    const float tl = __builtin_fmaf(beta, 6.42063831672e-09f, __builtin_fmaf(beta, 0.15915494309189535f, -th));
    u32 mx = umax(umax(asu(p_cre) & 0x7fffffffu, asu(p_cim) & 0x7fffffffu), umax(asu(p_cre2) & 0x7fffffffu, asu(p_cim2) & 0x7fffffffu));
    mx = dpp_max<0xB1, 0xf>(mx); mx = dpp_max<0x4E, 0xf>(mx); mx = dpp_max<0x124, 0xf>(mx); mx = dpp_max<0x128, 0xf>(mx);
    mx = dpp_max<0x142, 0xa>(mx); mx = dpp_max<0x143, 0xc>(mx);
    int ex = (int)(__builtin_amdgcn_readlane(mx, 63) >> 23) + 1;
    ex = ex < 32 ? 32 : (ex > 230 ? 230 : ex);
    const float cscale = asf((u32)(254 + 14 - ex) << 23);
    const float yscale = asf((u32)(ex - 17) << 23) * dtv;
    cpx z8c = zpow(a2, th, tl, 8.f);
    z8c.i = -z8c.i;
    u32 tgb[4];
    UNROLL for (int j = 0; j < 4; ++j) tgb[j] = ((tag >> (6 * j)) & 7u) | (((tag >> (6 * j + 3)) & 7u) << 16);
    u32 an[57];
#define KEEP(I, Z) do { const cpx z_ = (Z); an[I] = pk_f16(z_.r, z_.i); PIN(an[I]); } while (0)
    cpx e0[16], e1[16];
    cpx y; y.r = p_cre * cscale; y.i = -p_cim * cscale;
    UNROLL for (int i = 0; i < YB0; ++i) { KEEP(i, y); y = cmul(y, z8c); }
    HTS(16);
    HBAR();
    {
      const u32 stv = lds_ld<u32>(M_ST + 4 * (l & 7));
      v4u o[2];
      UNROLL for (int k = 0; k < 2; ++k) o[k] = cat(lds_tr(lnr + 128 * k), lds_tr(lnr + 128 * k + 4 * 1088));
      SB();
      UNROLL for (int i = YB0; i < YB0 + 4; ++i) { KEEP(i, y); y = cmul(y, z8c); }
      SB();
      UNROLL for (int k = 0; k < 2; ++k) {
        UNROLL for (int j = 0; j < 4; ++j) o[k][j] = (o[k][j] & ~0x00070007u) | tgb[j];
        __builtin_amdgcn_raw_buffer_store_b128(o[k], rws, xno + 1024 * k, 0, AUX_SC1);
      }
      if (tid < 8) __hip_atomic_store((GAS u64*)(ws + WS_ST) + 16 * rb + tid, ((u64)tag << 32) | stv, RLX_AGENT);
      HTS(1);
      UNROLL for (int i = YB0 + 4; i < YB1; ++i) { KEEP(i, y); if (i < 15) y = cmul(y, z8c); }
    }
    HTS(17);
    NBAR();
    UNROLL for (int i = YB1; i < 16; ++i) { KEEP(i, y); if (i < 15) y = cmul(y, z8c); }
    const cpx z1 = zpow(a2, th, tl, 1.f);
    e0[0].r = 32768.f; e0[0].i = 0.f;
    e0[1].r = z1.r * 32768.f; e0[1].i = z1.i * 32768.f;
    UNROLL for (int i = 2; i < 8; ++i) e0[i] = cmul(e0[i - 1], z1);
    UNROLL for (int i = 0; i < 8; ++i) KEEP(16 + i, e0[i]);
    HTS(18);
    NBAR();
    const cpx z128 = zpow(a2, th, tl, 128.f);
    e0[8].r = z128.r * 32768.f; e0[8].i = z128.i * 32768.f;
    UNROLL for (int i = 9; i < 16; ++i) e0[i] = cmul(e0[i - 8], z128);
    UNROLL for (int i = 8; i < 16; ++i) KEEP(16 + i, e0[i]);
    const cpx z256 = cmul(z128, z128), z512 = cmul(z256, z256);
    UNROLL for (int i = 0; i < 2; ++i) { e1[i] = cmul(e0[8 * (i >> 2) + 2 * (i & 3)], z256); KEEP(32 + i, e1[i]); }
    HTS(19);
    NBAR();
    UNROLL for (int i = 2; i < 8; ++i) { e1[i] = cmul(e0[8 * (i >> 2) + 2 * (i & 3)], z256); KEEP(32 + i, e1[i]); }
    UNROLL for (int i = 0; i < 4; ++i) { e1[8 + i] = cmul(e0[8 * (i >> 1) + 4 * (i & 1)], z512); KEEP(40 + i, e1[8 + i]); }
    UNROLL for (int i = 0; i < 4; ++i) { e1[12 + i] = cmul(e1[4 * (i >> 1) + 2 * (i & 1)], z512); KEEP(44 + i, e1[12 + i]); }
    HTS(20);
    NBAR();
    {
      const cpx z1024 = zpow(a2, th, tl, 1024.f);
      cpx e2;
      e2.r = z1024.r * 32768.f; e2.i = z1024.i * 32768.f;
      KEEP(48, e2); KEEP(49, cmul(e0[8], z1024)); KEEP(50, cmul(e1[0], z1024)); KEEP(51, cmul(e1[4], z1024));
      KEEP(52, cmul(e1[8], z1024)); KEEP(53, cmul(e1[10], z1024)); KEEP(54, cmul(e1[12], z1024)); KEEP(55, cmul(e1[14], z1024));
      KEEP(56, cmul(e2, z1024));
    }
    v4u uv[2];
    UNROLL for (int i = 0; i < 2; ++i) uv[i] = __builtin_amdgcn_raw_buffer_load_b128(rws, uoff + i * 32 * 8192, 0, AUX_SC1);
    HTS(21);
    NBAR();
    __builtin_amdgcn_s_setprio(HPRIO);
    const u32 kx = H_S + (hw | 1) * 4352 + 16 * l;
    f32x4 kacc[3];
    {
      v4u yb[4], xf[3][4];
      if (EARLY == 0) NBAR();
      UNROLL for (int i = 0; i < 16; ++i) lds_st<u32>(sw[i >> 2] + 272 * (i & 3), an[i]);
      UNROLL for (int s = 0; s < 4; ++s) yb[s] = lds_ld<v4u>(sr + 64 * s);
      UNROLL for (int t = 0; t < 3; ++t) {
        if (EARLY == t + 1) NBAR();
        UNROLL for (int i = 0; i < (t < 2 ? 16 : 9); ++i) lds_st<u32>(sw[i >> 2] + 272 * (i & 3), an[16 + 16 * t + i]);
        UNROLL for (int s = 0; s < 4; ++s) xf[t][s] = lds_ld<v4u>(sr + 64 * s);
        if (t > 0) {
          SB();
          kacc[t - 1] = f32x4{0.f, 0.f, 0.f, 0.f};
          UNROLL for (int s = 0; s < 4; ++s) kacc[t - 1] = mfma16h(xf[t - 1][s], yb[s], kacc[t - 1]);
          SB();
        }
      }
      kacc[2] = f32x4{0.f, 0.f, 0.f, 0.f};
      UNROLL for (int s = 0; s < 4; ++s) kacc[2] = mfma16h(xf[2][s], yb[s], kacc[2]);
    }
    TS(24);
#define U_PUT() do { UNROLL for (int i = 0; i < 2; ++i) { \
      _Pragma("unroll 1") for (int it = 0; it < SPIN_CAP; ++it) { \
        UNROLL for (int j = 0; j < 4; ++j) uv[i][j] ^= tgb[j]; \
        if ((((uv[i].x | uv[i].y) | (uv[i].z | uv[i].w)) & 0x00070007u) == 0) break; \
        __builtin_amdgcn_s_sleep(PSLP); \
        FENCE(); \
        uv[i] = __builtin_amdgcn_raw_buffer_load_b128(rws, uoff + i * 32 * 8192, 0, AUX_SC1); \
      } \
      lds_st<v4u>(uw + 512 * i, uv[i]); } } while (0)
    const u32 uw = H_U + (l & 1) * 4608 + 512 + (64 * hw + (l >> 1)) * 16;
    if (hm) {
      UNROLL for (int i = 0; i < 3; ++i) lds_st<f32x4>(kx + 1024 * i, kacc[i]);
      rec_post(hrec, hw, 2);
      if (l < 32) lds_st<v4u>(H_U + chl * 4608 + 16 * l, z4);
      rec_wait1(hrec, hw ^ 1, 2);
    } else {
      rec_wait1(hrec, hw ^ 1, 2);
      u32 kp[3][2];
      UNROLL for (int i = 0; i < 3; ++i) {
        const f32x4 v = (kacc[i] + lds_ld<f32x4>(kx + 1024 * i)) * 4.76837158203125e-07f;
        kp[i][0] = pk_f16(v[0], v[1]); kp[i][1] = pk_f16(v[2], v[3]);
      }
      lds_st<v2u>(ta0, v2u{kp[0][0], kp[0][1]});
      lds_st<u32>(ta1, kp[1][0]); lds_st<u32>(ta1b, kp[1][1]);
      lds_st<u16>(ta2, (u16)(g < 3 ? kp[2][0] : 0u));
      if (g < 2) { lds_st<u16>(ta2 + 32, (u16)(kp[2][0] >> 16)); lds_st<u16>(ta2 + 64, (u16)kp[2][1]); lds_st<u16>(ta2 + 96, (u16)(kp[2][1] >> 16)); }
      rec_post(hrec, hw, 2);
    }
    TS(3);
    const u32 eb = H_S + chl * 8704;
    {
      u32 kv[17];
      UNROLL for (int e = 0; e < 17; ++e) kv[e] = lds_ld<u16>(xa[e]);
      SB();
      U_PUT();
      SB();
      v4u ea0, ea1, eb0, eb1;
      UNROLL for (int e = 0; e < 4; ++e) {
        ea0[e] = kv[2 * e] | (kv[2 * e + 1] << 16); ea1[e] = kv[2 * e + 8] | (kv[2 * e + 9] << 16);
        eb0[e] = kv[2 * e + 1] | (kv[2 * e + 2] << 16); eb1[e] = kv[2 * e + 9] | (kv[2 * e + 10] << 16);
      }
      const u32 a = eb + 32 * (64 * hm + l);
      lds_st<v4u>(a, ea0); lds_st<v4u>(a + 16, ea1); lds_st<v4u>(a + 4160, eb0); lds_st<v4u>(a + 4176, eb1);
      if (l < 4 && hm == 0) lds_st<v4u>(eb + 4096 + (l >> 1) * 4160 + 16 * (l & 1), z4);
    }
    rec_post(hrec, hw, 4);
    u32 fa4[4];
    {
      const int x0 = 2047 - 16 * (1 + 8 * hm) - i16 + 8 * g;
      const u32 a = eb + ((i16 & 1) ? 0 : 4160) + 4 * (x0 >> 1) - 512 * 7 - 192;
      UNROLL for (int i = 0; i < 4; ++i) { fa4[i] = a + 1024 * (3 - i); PIN(fa4[i]); }
    }
#define FA_LD(W, MI) cat(*(const LAS v2u_a4*)(uintptr_t)(fa4[(W) >> 1] + 512 * (1 - ((W) & 1)) + 192 - 64 * (MI)), \
                         *(const LAS v2u_a4*)(uintptr_t)(fa4[(W) >> 1] + 512 * (1 - ((W) & 1)) + 192 - 64 * (MI) + 8))
    v4u FA[4][8], G[4][8];
    TS(4);
    rec_wait4(hrec, 4);
    if (b == 0 && tid == 0) __hip_atomic_store((GAS u32*)(ws + WS_NONCE), tag, RLX_AGENT);
    TS(5);
    f32x4 acc[8];
    UNROLL for (int i = 0; i < 8; ++i) acc[i] = f32x4{0.f, 0.f, 0.f, 0.f};
    const u32 ub = H_U + chl * 4608 + 512;
    v2u uu[4];
    u32 pf;
    {
      const u32 a = ub + 32 * i16 + 16 * g - 32 * (1 + 8 * hm) - 192;
      G[0][0] = lds_ld<v4u>(a + 192);
      UNROLL for (int w = 0; w < 8; ++w) FA[0][w] = FA_LD(w, 0);
      UNROLL for (int v = 1; v < 8; ++v) G[0][v] = lds_ld<v4u>(a + 512 * v + 192);
      UNROLL for (int mi = 0; mi < 4; ++mi) {
        UNROLL for (int v = 0; v < 8; ++v) {
          SB();
          UNROLL for (int w = 0; v + w < 8; ++w) {
            acc[v + w] = mfma16h(FA[mi][w], G[mi][v], acc[v + w]);
            if (w == 0 && mi < 3) { SB(); G[mi + 1][v] = lds_ld<v4u>(a + 512 * v + 192 - 64 * (mi + 1)); SB(); }
            if (mi < 3 && v < 4 && (w == 1 || w == 3)) { SB(); FA[mi + 1][2 * v + (w >> 1)] = FA_LD(2 * v + (w >> 1), mi + 1); SB(); }
            if (mi == 3 && w == 0 && v < 4) { SB(); uu[v] = lds_ld<v2u>(ub + 2 * (256 * (4 * hm + v) + 16 * i16 + 4 * g)); SB(); }
            if (mi == 3 && w == 0 && v == 4) { SB(); pf = *(volatile LAS u32*)(uintptr_t)(hrec + 4 * (hw ^ 1)); SB(); }
          }
          if (mi == 2 && v == 4) rec_post(hrec, hw, 5);
        }
      }
    }
    TS(6);
    {
      const u32 xw = H_S + hw * 4352 + 16 * l, xrd = H_S + (hw ^ 1) * 4352 + 16 * l;
      if (!all64(pf >= 5)) rec_wait1(hrec, hw ^ 1, 5);
      FENCE();
      UNROLL for (int i = 0; i < 4; ++i) { const f32x4 lo = acc[i], hi = acc[4 + i]; lds_st<f32x4>(xw + 1024 * i, hm ? lo : hi); acc[i] = hm ? hi : lo; }
      rec_post(hrec, hw, 6);
      const float dsv = p_dsk * (1.0f / 1024.0f);
      u32 yo = WS_Y + ch * 4096 + hm * 2048 + 16 * l;
      PIN(yo);
      UNROLL for (int i = 0; i < 4; ++i) {
        const u32 ux = uu[i].x, uy = uu[i].y;
        const f16x2 u01 = __builtin_bit_cast(f16x2, ux), u23 = __builtin_bit_cast(f16x2, uy);
        acc[i][0] = __builtin_fmaf((float)u01.x, dsv, acc[i][0] * yscale); acc[i][1] = __builtin_fmaf((float)u01.y, dsv, acc[i][1] * yscale);
        acc[i][2] = __builtin_fmaf((float)u23.x, dsv, acc[i][2] * yscale); acc[i][3] = __builtin_fmaf((float)u23.y, dsv, acc[i][3] * yscale);
        PIN(acc[i]);
      }
      rec_wait1(hrec, hw ^ 1, 6);
      u32 o[4][2];
      UNROLL for (int i = 0; i < 4; ++i) {
        const f32x4 y = lds_ld<f32x4>(xrd + 1024 * i);
        o[i][0] = pk_bf16(__builtin_fmaf(y[0], yscale, acc[i][0]), __builtin_fmaf(y[1], yscale, acc[i][1]));
        o[i][1] = pk_bf16(__builtin_fmaf(y[2], yscale, acc[i][2]), __builtin_fmaf(y[3], yscale, acc[i][3]));
      }
      UNROLL for (int i = 0; i < 4; i += 2) {
        const auto s0 = __builtin_amdgcn_permlane16_swap(o[i][0], o[i + 1][0], false, false);
        const auto s1 = __builtin_amdgcn_permlane16_swap(o[i][1], o[i + 1][1], false, false);
        const v4u v = {s0[0], s1[0], s0[1], s1[1]};
        __builtin_amdgcn_raw_buffer_store_b128(v, rws, yo + (i >> 1) * 1024, 0, AUX_SC1);
      }
      rec_post(hrec, hw, 7);
      TS(2);
      WAIT_V(0);
      if (l == 0) __hip_atomic_store((GAS u32*)(ws + WS_FY) + FL * (4 * (c0 >> 1) + hw), tag, RLX_AGENT);
    }
    TS(7);
    xo = __builtin_amdgcn_raw_buffer_load_b128(RSRC(PTR(ka, 0), L * D * 2), ooff, 0, 0);
    if (hw == 0) lds_st<float>(M_RS + 4 * l, __builtin_amdgcn_rcpf(bf_hi(poll_granule((const GAS u64*)(ws + WS_ST) + 16 * (8 * tt + (l >> 3)) + (l & 7), tag))));
    poll_flag((const GAS u32*)(ws + WS_FY) + FL * (4 * (32 * dt + 8 * hw + (i16 >> 1)) + 2 * (l & 1) + (tt >> 4)), tag);
    AFTER_POLL();
    TS(8);
    {
      v4u yv[2];
      const int pc = l & 7;
      UNROLL for (int i = 0; i < 2; ++i)
        yv[i] = __builtin_amdgcn_raw_buffer_load_b128(rws, WS_Y + (64 * dt + 16 * hw + 8 * i + (l >> 3)) * 4096 + (tt >> 3) * 1024 +
                                                     (2 * (pc >> 2) + ((tt >> 2) & 1)) * 256 + (4 * (tt & 3) + (pc & 3)) * 16, 0, AUX_SC1);
      rec_wait4(hrec, 7);
      const u32 yst = H_S + hw * 4352;
      UNROLL for (int i = 0; i < 2; ++i) lds_st<v4u>(yst + (8 * i + (l >> 3)) * 192 + 32 * (pc & 3) + 16 * (pc >> 2), yv[i]);
      const u32 a = yst + (i16 >> 2) * 192 + 32 * g + 8 * (l & 3);
      const v4u y0 = cat(lds_tr(a), lds_tr(a + 4 * 192)), y1 = cat(lds_tr(a + 8 * 192), lds_tr(a + 12 * 192));
      lds_st<v4u>(H_U + l * 144 + 32 * hw, y0);
      lds_st<v4u>(H_U + l * 144 + 32 * hw + 16, y1);
    }
    TS(9);
  } else {
    __builtin_amdgcn_s_setprio(PRIO);
    const int w = wave - 4, wf = w & 1, wt = w >> 1;
    const int h = l >> 5, r32 = l & 31, blk = (l >> 4) & 1, q = (l & 15) >> 2, p = l & 3;
    const int mt = 4 * (xj >> 1) + (r & 3), nt = 7 * (xj & 1) + (r >> 2);
    u32 v_x[4], v_w1[4], v_l = 16 * l;
    UNROLL for (int i = 0; i < 4; ++i) {
      v_x[i] = (l >> 3) * 1024 + (((l & 7) ^ (l >> 4) ^ (4 * (i & 1))) * 16) + i * 7168;
      v_w1[i] = (l >> 4) * 3584 + (((l & 15) ^ ((l >> 4) << 2)) * 16) + i * (4 * 3584 - 1024);
      PIN(v_x[i]); PIN(v_w1[i]);
    }
    u32 s_x = (128 * mt + 32 * w) * 1024, s_w1 = (16 * w) * 3584 + 256 * nt, s_r = (256 * xj + 8 * r + 2 * w) * 1024;
    const int iu = 4 * (xj >> 1) + r - 28, ii = 14 * (xj & 1) + (iu < 14 ? iu : 0);
    asm volatile("" : "+s"(s_x), "+s"(s_w1), "+s"(s_r));
    PIN(v_l);
    ARGS_WAIT();
    TS(25);
    char* const ws = PTR(kc, 0);
    const v4i sx = {ka[0], ka[1], L * D * 2, 0x00020000}, sw1 = {ka[14], ka[15], D * F * 2, 0x00020000}, sws = {kc[0], kc[1], WS_END, 0x00020000};
    v4u xr[2];
    asm volatile("buffer_load_dwordx4 %0, %2, %3, %4 offen\n\tbuffer_load_dwordx4 %1, %2, %3, %4 offen offset:1024"
                 : "=&v"(xr[0]), "=&v"(xr[1]) : "v"(v_l), "s"(sx), "s"(s_r) : "memory");
    u32 so;
#define SET_M0(V) asm volatile("s_mov_b32 m0, %1" : "+s"(so) : "s"(V) : "memory")
#define GLDS(VO, SRD, I) asm volatile("buffer_load_dwordx4 %0, %1, %2 offen offset:%3 lds" ::"v"(VO), "s"(SRD), "s"(so), "n"((I) * 1024) : "memory")
#define SETG(N) do { const int G_ = (N) >> 2, T_ = G_ >> 1; \
        if (G_ & 1) { so = s_w1 + T_ * (64 * 3584); SET_M0((T_ & 3) * 32768 + 16384 + w * 4096); } \
        else { so = s_x + T_ * 128; SET_M0((T_ & 3) * 32768 + w * 4096); } } while (0)
#define GL(N) do { if (((N) >> 2) & 1) GLDS(v_w1[(N) & 3], sw1, (N) & 3); else GLDS(v_x[(N) & 3], sx, (N) & 3); } while (0)
#define GLRUN(N0, N1) do { UNROLL for (int n_ = (N0); n_ < (N1); ++n_) { if ((n_ & 3) == 0) { SETG(n_); asm volatile("s_nop 0"); } GL(n_); } } while (0)
    v4u wv[16];
    const u32 v_c = (l >> 3) * 3584 + (l & 7) * 16;
#define CSRUN(N0, N1) do { UNROLL for (int i_ = (N0); i_ < (N1); ++i_) { const u32 sof_ = (128 * w + 8 * i_) * 3584 + 128 * ii; \
        asm volatile("buffer_load_dwordx4 %0, %1, %2, %3 offen" : "=&v"(wv[i_]) : "v"(v_c), "s"(sw1), "s"(sof_) : "memory"); } } while (0)
    if (r < 28) GLRUN(0, NPRE); else CSRUN(0, NPRE);
    const __amdgpu_buffer_rsrc_t rws = RSRC(ws, WS_END), rw2 = RSRC(PTR(kb, 0), F * D * 2);
    const u32 v_w2 = (l >> 3) * 1024 + (l & 7) * 16;
    v4u wr[56];
#define RAW(I) wr[I] = __builtin_amdgcn_raw_buffer_load_b128(rw2, v_w2, (448 * w + 8 * (I)) * 1024 + 128 * dt, 0)
    u32 ab[2][4], aa[2][2];
    {
      const int row = 64 * wt + r32, z = (row >> 1) & 7;
      UNROLL for (int s = 0; s < 4; ++s) { ab[0][s] = row * 128 + (((2 * s + h) ^ z) * 16); ab[1][s] = ab[0][s] + 65536; PIN(ab[0][s]); PIN(ab[1][s]); }
      UNROLL for (int f2 = 0; f2 < 2; ++f2) {
        aa[0][f2] = 16384 + (8 * h + q) * 256 + (((8 * wf + 4 * f2 + 2 * blk + (p >> 1)) ^ (q << 2)) * 16) + 8 * (p & 1);
        aa[1][f2] = aa[0][f2] + 65536; PIN(aa[0][f2]); PIN(aa[1][f2]);
      }
    }
    u32 wa[2], lnw = H_U + 2 * w * 1088 + 16 * l;
    wa[0] = w * 8192 + (l >> 3) * 128 + (((l & 7) ^ (((l >> 4) & 1) << 2)) * 16); wa[1] = wa[0] + 65536;
    PIN(wa[0]); PIN(wa[1]); PIN(lnw);
#define W2_ST(I) lds_st<v4u>(wa[((I) >> 4) & 1] + (((I) >> 3) & 1) * 32768 + ((I) & 7) * 1024, wr[I])
    TS(9);
    asm volatile("s_waitcnt vmcnt(%2)" : "+v"(xr[0]), "+v"(xr[1]) : "n"(NPRE) : "memory");
    TS(12);
    {
      float s[2], qq[2], d[2][8], m[2], var[2];
      UNROLL for (int k = 0; k < 2; ++k) {
        s[k] = 0.f; qq[k] = 0.f;
        UNROLL for (int j = 0; j < 4; ++j) { const u32 u = xr[k][j]; s[k] = dot2(u, 0x3f803f80u, s[k]); qq[k] = dot2(u, u, qq[k]); }
      }
#define RED4(C, RM) do { s[0] = dpp_add<C, RM>(s[0]); qq[0] = dpp_add<C, RM>(qq[0]); s[1] = dpp_add<C, RM>(s[1]); qq[1] = dpp_add<C, RM>(qq[1]); } while (0)
      RED4(0xB1, 0xf); RED4(0x4E, 0xf); RED4(0x124, 0xf); RED4(0x128, 0xf); RED4(0x142, 0xa); RED4(0x143, 0xc);
      SB();
      if (r < 28) GLRUN(NPRE, NPRE + NM1); else CSRUN(NPRE, 16);
      SB();
      float e1[2];
      bool redo = false;
      UNROLL for (int k = 0; k < 2; ++k) {
        e1[k] = asf(__builtin_amdgcn_readlane(asu(s[k]), 63)) * (1.0f / D);
        const float e2 = asf(__builtin_amdgcn_readlane(asu(qq[k]), 63)) * (1.0f / D);
        var[k] = __builtin_fmaf(-e1[k], e1[k], e2);
        redo |= !(var[k] * 16.f >= e2);
        m[k] = rbf(e1[k]);
        UNROLL for (int j = 0; j < 4; ++j) { const u32 u = xr[k][j]; d[k][2 * j] = bf_lo(u) - m[k]; d[k][2 * j + 1] = bf_hi(u) - m[k]; }
      }
      if (__builtin_amdgcn_readfirstlane(redo)) {
        UNROLL for (int k = 0; k < 2; ++k) {
          s[k] = 0.f; qq[k] = 0.f;
          UNROLL for (int j = 0; j < 8; ++j) { s[k] += d[k][j]; qq[k] = __builtin_fmaf(d[k][j], d[k][j], qq[k]); }
        }
        RED4(0xB1, 0xf); RED4(0x4E, 0xf); RED4(0x124, 0xf); RED4(0x128, 0xf); RED4(0x142, 0xa); RED4(0x143, 0xc);
        UNROLL for (int k = 0; k < 2; ++k) {
          const float c1 = asf(__builtin_amdgcn_readlane(asu(s[k]), 63)) * (1.0f / D), c2 = asf(__builtin_amdgcn_readlane(asu(qq[k]), 63)) * (1.0f / D);
          var[k] = __builtin_fmaxf(__builtin_fmaf(-c1, c1, c2), 0.f);
        }
      }
      u32 pay[2];
      UNROLL for (int k = 0; k < 2; ++k) {
        const float sd = rbf(__builtin_amdgcn_sqrtf(rbf(rbf(var[k]) + 1e-5f)));
        const float rs = 1024.0f * __builtin_amdgcn_rcpf(sd);
        v4u o;
        UNROLL for (int j = 0; j < 4; ++j) {
          const u32 dd = pk_bf16(d[k][2 * j], d[k][2 * j + 1]);
          const u32 n = pk_bf16(bf_lo(dd) * rs, bf_hi(dd) * rs);
          o[j] = pk_f16(bf_lo(n), bf_hi(n));
        }
        lds_st<v4u>(lnw + k * 1088, o);
        pay[k] = (asu(m[k]) >> 16) | (asu(sd) & 0xffff0000u);
        if (k == 0) { SB(); if (r < 28) GLRUN(NPRE + NM1, NPRE + NM1 + NM2); SB(); }
      }
      lds_st<v2u>(M_ST + 8 * w, v2u{pay[0], pay[1]});
    }
    SB();
    TS(13);
    u32 tag;
    constexpr int NRAW0 = RAW_BEFORE(28) + NRL;
    constexpr int NJOIN = NRAW0 + NB;
    if (r < 28) {
      GLRUN(NPRE + NM1 + NM2, NPRO);
      const u32 crec = M_CF + 16 * l;
      f32x16 acc[2][2];
      UNROLL for (int i = 0; i < 2; ++i) UNROLL for (int j = 0; j < 2; ++j) UNROLL for (int e = 0; e < 16; ++e) acc[i][j][e] = 0.f;
      v4u fb[2][4][2];
      v2u fl[2][4][2], fh[2][4][2];
      v2u gm[2], gc[2];
#define RD(T, s_, j_) do { const int hi_ = ((T) >> 1) & 1, o_ = ((T) & 1) * 32768, u_ = (T) & 1; \
        if ((j_) == 0) fb[u_][s_][0] = lds_ld<v4u>(ab[hi_][s_] + o_); \
        if ((j_) == 1) fl[u_][s_][0] = lds_tr(aa[hi_][0] + o_ + (s_) * 4096); \
        if ((j_) == 2) fh[u_][s_][0] = lds_tr(aa[hi_][0] + o_ + (s_) * 4096 + 1024); \
        if ((j_) == 3) fb[u_][s_][1] = lds_ld<v4u>(ab[hi_][s_] + o_ + 4096); \
        if ((j_) == 4) fl[u_][s_][1] = lds_tr(aa[hi_][1] + o_ + (s_) * 4096); \
        if ((j_) == 5) fh[u_][s_][1] = lds_tr(aa[hi_][1] + o_ + (s_) * 4096 + 1024); } while (0)
#define RDN(T, J0, N) do { UNROLL for (int j2_ = (J0); j2_ < (J0) + (N); ++j2_) { \
        if ((T) < 7) RD(T, j2_ / 6, j2_ % 6); \
        else if (j2_ < 12) RD(T, j2_ / 3, j2_ % 3); else if (j2_ < 16) RD(T, (j2_ - 12) & 3, 3); else RD(T, ((j2_ - 16) >> 1) & 3, 4 + (j2_ & 1)); } } while (0)
#define MM(T, S, I, J) acc[I][J] = mfma32(cat(fl[(T) & 1][S][I], fh[(T) & 1][S][I]), fb[(T) & 1][S][J], acc[I][J])
#define SLOT(K, J) do { if ((K) < ND0) { GL(NPRO + 4 * (K) + 2 * (J)); GL(NPRO + 4 * (K) + 2 * (J) + 1); } \
        else if (IS_D(K)) GL(26 + 2 * (K) + (J)); else RAW(RAW_BEFORE(K) + (J)); } while (0)
#define NEXTG(K) do { if ((K) < ND0) SETG(NPRO + 4 * (K)); else if (IS_D(K) && ((K) & 1)) SETG(26 + 2 * (K)); } while (0)
      NEXTG(0);
      u32 v_g = r32 * 8, v_m = (r32 >> 3) * 128 + (r32 & 7) * 8, ho[2];
      UNROLL for (int j = 0; j < 2; ++j) { ho[j] = WS_H + ((4 * mt + 2 * wt + j) * 112 + 8 * nt + 4 * wf) * 1024 + 16 * l; PIN(ho[j]); }
      PIN(v_g); PIN(v_m);
      TS(16);
      BAR_V(NPRO - 8);
      TS(1);
      RDN(0, 0, 10);
      v4u ca[2], cb[2], cf;
      u32 tagv;
      SFOR(k, 0, 28)
        constexpr int t = k >> 2, s = k & 3;
        SB();
        MM(t, s, 0, 0);
        SB();
        if (s == 3) {
          if (t < 5) BAR_V(BEFORE(k) - LASTPOS(t + 1) - 1);
          if (t == 5) BAR_V(BEFORE(k) - LASTPOS(7) - 1);
          if (CTS || t == 3 || t == 5 || t == 6) TS(17 + t);
          RDN(t + 1, 0, 2);
        }
        SLOT(k, 0);
        SB();
        MM(t, s, 0, 1);
        SB();
        if (s == 3) RDN(t + 1, 2, 4);
        if (s == 0) RDN(t, 10, 4);
        if (s == 1) RDN(t, 17, 4);
        if (k == 2) tagv = lds_ld<u32>(M_TAG);
        SB();
        MM(t, s, 1, 0);
        SB();
        SLOT(k, 1);
        SB();
        MM(t, s, 1, 1);
        SB();
        if (s == 3) RDN(t + 1, 6, 4);
        if (s == 0) RDN(t, 14, 3);
        if (s == 1) RDN(t, 21, 3);
        if (k == KGRAN) {
          const u32 so_m = WS_ST + (16 * mt + 8 * wt) * 128, so_c = WS_CS + (128 * nt + 64 * wf) * 8;
          asm volatile("buffer_load_dwordx2 %0, %8, %5, %6 offen sc1\n\tbuffer_load_dwordx2 %1, %8, %5, %6 offen offset:512 sc1\n\t"
                       "buffer_load_dwordx2 %2, %4, %5, %7 offen sc1\n\tbuffer_load_dwordx2 %3, %4, %5, %7 offen offset:256 sc1"
                       : "=&v"(gm[0]), "=&v"(gm[1]), "=&v"(gc[0]), "=&v"(gc[1]) : "v"(v_g), "s"(sws), "s"(so_m), "s"(so_c), "v"(v_m) : "memory");
        }
        if (k == 6) tag = __builtin_amdgcn_readfirstlane(tagv);
        NEXTG(k + 1);
      SEND
      v4u ho_;
#define CVT(I, J, S, E) do { const s16x2 v_ = __builtin_bit_cast(s16x2, pk_bf16(acc[I][J][8 * (S) + 2 * (E)], acc[I][J][8 * (S) + 2 * (E) + 1])); \
        const s16x2 z_ = {0, 0}; ho_[E] = __builtin_bit_cast(u32, __builtin_elementwise_max(v_, z_)); } while (0)
#define HST(I, J, S) __builtin_amdgcn_raw_buffer_store_b128(ho_, rws, ho[J] + (2 * (I) + (S)) * 1024, 0, AUX_SC1)
      UNROLL for (int m = 0; m < 25; ++m) {
        const int a = m / 5, s = m % 5, pa_ = a - 1;
        SB();
        if (a < 4) { if (s < 4) MM(7, s & 3, (a >> 1) & 1, a & 1); else acc[(a >> 1) & 1][a & 1] = mfma32(ca[(a >> 1) & 1], cb[a & 1], acc[(a >> 1) & 1][a & 1]); }
        SB();
        if (m == 0) RDN(7, 10, 4);
        if (m == 1) {
          asm volatile("s_waitcnt vmcnt(%4)" : "+v"(gm[0]), "+v"(gm[1]), "+v"(gc[0]), "+v"(gc[1]) : "n"(BEFORE(28) - GPOS - 1 + (NRL > 0 ? 1 : 0)) : "memory");
          u32 bad = ((gm[0].y ^ tag) | (gm[1].y ^ tag)) | ((gc[0].y ^ tag) | (gc[1].y ^ tag));
          PIN(bad);
          if (__builtin_expect(__builtin_amdgcn_ballot_w64(bad != 0) != 0, 0)) {
            const GAS u64* pm = (const GAS u64*)(ws + WS_ST) + 16 * (16 * mt + 8 * wt + (r32 >> 3)) + (r32 & 7);
            const GAS u64* pc = (const GAS u64*)(ws + WS_CS) + 128 * nt + 64 * wf + r32;
#pragma unroll 1
            for (int it = 0; it < SPIN_CAP; ++it) {
              const u64 m0_ = __hip_atomic_load(pm, RLX_AGENT), m1_ = __hip_atomic_load(pm + 64, RLX_AGENT);
              const u64 c0_ = __hip_atomic_load(pc, RLX_AGENT), c1_ = __hip_atomic_load(pc + 32, RLX_AGENT);
              gm[0].x = (u32)m0_; gm[1].x = (u32)m1_; gc[0].x = (u32)c0_; gc[1].x = (u32)c1_;
              if (all64((u32)(m0_ >> 32) == tag && (u32)(m1_ >> 32) == tag && (u32)(c0_ >> 32) == tag && (u32)(c1_ >> 32) == tag)) break;
            }
          }
        }
        if (m == 2) { ca[0] = v4u{h ? 0u : gc[0].x, 0u, 0u, 0u}; PIN(ca[0]); }
        if (m == 2) { cb[0] = v4u{__builtin_amdgcn_perm(gm[0].x, gm[0].x, 0x01000100u) ^ 0x80008000u, 0u, 0u, 0u}; PIN(cb[0]); }
        if (m == 3) RDN(7, 14, 4);
        if (m == 4) { cb[1] = v4u{__builtin_amdgcn_perm(gm[1].x, gm[1].x, 0x01000100u) ^ 0x80008000u, 0u, 0u, 0u}; PIN(cb[1]); }
        if (m == 5) { RDN(7, 18, 6); rec_post(crec, w, 2); }
        if (m == 10) { ca[1] = v4u{h ? 0u : gc[1].x, 0u, 0u, 0u}; PIN(ca[1]); }
        if (m == 15) cf = *(volatile LAS v4u*)(uintptr_t)crec;
        if (m < NRL) RAW(m < NRL ? RAW_BEFORE(28) + m : 0);
        if (pa_ >= 0 && s >= 1) {
          const int I = (pa_ >> 1) & 1, J = pa_ & 1, S = (s - 1) >> 1, E = 2 * ((s - 1) & 1);
          CVT(I, J, S, E); CVT(I, J, S, E + 1);
          if (E == 2) HST(I, J, S);
        }
      }
      static_assert(NRL <= 7 && NJOIN <= 56 - NFILL && NJOIN >= 28 && NRAW0 <= 32, "w2 schedule");
      SB();
      TS(10);
      if (!all64(umin(umin(cf.x, cf.y), umin(cf.z, cf.w)) >= 2u)) rec_wait4(crec, 2);
      FENCE();
      UNROLL for (int i = 0; i < NRAW0; ++i) W2_ST(i);
      SB();
      UNROLL for (int i = NRAW0; i < NJOIN; ++i) RAW(i);
      SB();
      TS(3);
      WAIT_V(NB);
      if (l == 0) __hip_atomic_store((GAS u32*)(ws + WS_FH) + FL * ((2 * mt + wt) * 32 + 2 * nt + wf), tag, RLX_AGENT);
      TS(4);
    } else {
      __builtin_amdgcn_s_setprio(0);
      u32 tch[7];
      const u32 v_t = (l >> 2) * 1024 + (l & 3) * 128;
#define TOUCH(I) tch[I] = __builtin_amdgcn_raw_buffer_load_b32(rw2, v_t, (448 * (r - 28) + 64 * (I) + 16 * w) * 1024, 0)
      TOUCH(0); RAW(0); RAW(1); RAW(2); RAW(3);
      HBAR();
      asm volatile("s_waitcnt vmcnt(5)" : "+v"(wv[0]), "+v"(wv[1]), "+v"(wv[2]), "+v"(wv[3]), "+v"(wv[4]), "+v"(wv[5]), "+v"(wv[6]), "+v"(wv[7]),
                   "+v"(wv[8]), "+v"(wv[9]), "+v"(wv[10]), "+v"(wv[11]), "+v"(wv[12]), "+v"(wv[13]), "+v"(wv[14]), "+v"(wv[15])::"memory");
      TOUCH(1); RAW(4); RAW(5); RAW(6); RAW(7);
      SB();
      float cs[8];
      u32 one_lo = 0x00003f80u, one_hi = 0x3f800000u;
      PIN(one_lo); PIN(one_hi);
      UNROLL for (int j = 0; j < 8; ++j) cs[j] = 0.f;
      UNROLL for (int i = 0; i < 16; ++i) UNROLL for (int j = 0; j < 4; ++j) { const u32 u = wv[i][j]; cs[2 * j] = dot2(u, one_lo, cs[2 * j]); cs[2 * j + 1] = dot2(u, one_hi, cs[2 * j + 1]); }
      lds_st<f32x4>(98304 + (8 * w + (l >> 3)) * 272 + 32 * (l & 7), f32x4{cs[0], cs[1], cs[2], cs[3]});
      lds_st<f32x4>(98304 + (8 * w + (l >> 3)) * 272 + 32 * (l & 7) + 16, f32x4{cs[4], cs[5], cs[6], cs[7]});
      HBAR();
      tag = __builtin_amdgcn_readfirstlane(lds_ld<u32>(M_TAG));
      {
        float s = 0.f;
        UNROLL for (int i = 0; i < 8; ++i) s += lds_ld<float>(98304 + (8 * (l >> 4) + i) * 272 + 4 * (16 * w + (l & 15)));
        const auto s16 = __builtin_amdgcn_permlane16_swap(asu(s), asu(s), false, false);
        s = asf(s16[0]) + asf(s16[1]);
        const auto s32 = __builtin_amdgcn_permlane32_swap(asu(s), asu(s), false, false);
        s = asf(s32[0]) + asf(s32[1]);
        const float hi = rbf(s);
        if (iu < 14 && l < 16) __hip_atomic_store((GAS u64*)(ws + WS_CS) + 64 * ii + 16 * w + l, ((u64)tag << 32) | pk_bf16(hi, s - hi), RLX_AGENT);
      }
      TS(2);
      UNROLL for (int i = 2; i < 7; ++i) { TOUCH(i); RAW(4 * i); RAW(4 * i + 1); RAW(4 * i + 2); RAW(4 * i + 3); NBAR(); }
      UNROLL for (int i = 0; i < NRAW0; ++i) { if (28 + i < NJOIN) RAW(28 + i < NJOIN ? 28 + i : 0); SB(); W2_ST(i); SB(); }
      UNROLL for (int i = 0; i < 7; ++i) asm volatile("" ::"v"(tch[i]));
      __builtin_amdgcn_s_waitcnt(0x0F70);
      SB();
    }
    u32 a2[2][2];
    UNROLL for (int d2 = 0; d2 < 2; ++d2) {
      a2[0][d2] = w * 8192 + (4 * h + q) * 128 + (((4 * d2 + 2 * blk + (p >> 1)) ^ (((q >> 1) & 1) << 2)) * 16) + 8 * (p & 1);
      a2[1][d2] = a2[0][d2] + 65536; PIN(a2[0][d2]); PIN(a2[1][d2]);
    }
#define W2_FRAG(K, D2) cat(lds_tr(a2[((K) >> 3) & 1][D2] + (((K) >> 2) & 1) * 32768 + ((K) & 3) * 2048), lds_tr(a2[((K) >> 3) & 1][D2] + (((K) >> 2) & 1) * 32768 + ((K) & 3) * 2048 + 1024))
    UNROLL for (int i = NJOIN; i < 56 - NFILL; ++i) { RAW(i); SB(); if (i >= NJOIN + 4 && i - NJOIN - 4 + NRAW0 < 32) { W2_ST(i - NJOIN - 4 + NRAW0); SB(); } }
    static_assert(56 - NFILL - NJOIN - 4 + NRAW0 >= 32, "w2 schedule");
    TS(5);
    const GAS u32* fhp = (const GAS u32*)(ws + WS_FH) + FL * (tt * 32 + 7 * w + (l < 7 ? l : 0));
    const u32 fh0 = __hip_atomic_load(fhp, RLX_AGENT);
    SB();
    UNROLL for (int i = 56 - NFILL; i < 56; ++i) RAW(i);
    SB();
    v4u fa[28][2];
    UNROLL for (int k = 0; k < 2; ++k) { fa[k][0] = W2_FRAG(k, 0); fa[k][1] = W2_FRAG(k, 1); }
    u32 s_h = WS_H + ((2 * tt) * 112 + 28 * w) * 1024;
    u32 v_h[2] = {(u32)(16 * l), (u32)(16 * l + 112 * 1024)};
    asm volatile("" : "+s"(s_h));
    PIN(v_h[0]); PIN(v_h[1]);
    SB();
    TS(11);
    if (!all64(fh0 == tag)) poll_flag(fhp, tag);
    AFTER_POLL();
    TS(6);
    f32x16 acc[2][2];
    UNROLL for (int i = 0; i < 2; ++i) UNROLL for (int j = 0; j < 2; ++j) UNROLL for (int e = 0; e < 16; ++e) acc[i][j][e] = 0.f;
    v4u hb[28][2];
#define H_LOAD(K, T2) hb[K][T2] = __builtin_amdgcn_raw_buffer_load_b128(rws, v_h[T2] + ((K) & 3) * 1024, s_h + ((K) >> 2) * 4096, AUX_SC1)
    UNROLL for (int k = 0; k < HWIN; ++k) { H_LOAD(k, 0); SB(); H_LOAD(k, 1); SB(); }
    SB();
    UNROLL for (int k = 0; k < 24; ++k) {
      const int kn = k + 2, st = 32 + 2 * (k - 2);
      acc[0][0] = mfma32(fa[k][0], hb[k][0], acc[0][0]);
      SB();
      fa[kn][0] = W2_FRAG(kn, 0);
      if (k >= 22) fa[kn + 2][0] = W2_FRAG(kn + 2, 0);
      SB();
      acc[1][0] = mfma32(fa[k][1], hb[k][0], acc[1][0]);
      SB();
      if (k + HWIN < 28) H_LOAD(k + HWIN, 0);
      if (k >= 2 && k < 14) W2_ST(k >= 2 && k < 14 ? st : 32);
      SB();
      acc[0][1] = mfma32(fa[k][0], hb[k][1], acc[0][1]);
      SB();
      fa[kn][1] = W2_FRAG(kn, 1);
      if (k >= 22) fa[kn + 2][1] = W2_FRAG(kn + 2, 1);
      SB();
      acc[1][1] = mfma32(fa[k][1], hb[k][1], acc[1][1]);
      SB();
      if (k + HWIN < 28) H_LOAD(k + HWIN, 1);
      if (k >= 2 && k < 14) W2_ST(k >= 2 && k < 14 ? st + 1 : 32);
      if (k == 0) TS(26);

      if (k == 14) xo = __builtin_amdgcn_raw_buffer_load_b128(RSRC(PTR(ka, 0), L * D * 2), ooff, 0, 0);
      SB();
    }
    TS(7);
    u32 pw[2][2];
    UNROLL for (int i = 0; i < 2; ++i) UNROLL for (int e = 0; e < 2; ++e) { pw[i][e] = w * 8192 + r32 * 256 + (((4 * i + 2 * e + h) ^ (r32 & 7)) * 16); PIN(pw[i][e]); }
    UNROLL for (int m = 0; m < 21; ++m) {
      const int a = m >> 2, mp = m - 5, pa_ = mp >> 2, e = mp & 3;
      SB();
      if (a < 4) acc[a & 1][(a >> 1) & 1] = mfma32(fa[24 + (m & 3)][a & 1], hb[24 + (m & 3)][(a >> 1) & 1], acc[a & 1][(a >> 1) & 1]);
      SB();
      if (mp >= 0) {
        const int i = pa_ & 1, j = (pa_ >> 1) & 1;
        const f32x4 v = {acc[i][j][4 * e], acc[i][j][4 * e + 1], acc[i][j][4 * e + 2], acc[i][j][4 * e + 3]};
        lds_st<f32x4>(pw[e >> 1][e & 1] + j * 32768 + i * 128, v);
      }
    }
    TS(8);
  }
  BAR_L();
  TS(14);
  {
    const int tok = tid >> 3, o = tid & 7;
    const u32 a = (tok >> 5) * 32768 + (tok & 31) * 256;
    const u32 c0a = a + (((2 * o) ^ (tok & 7)) * 16), c1a = a + (((2 * o + 1) ^ (tok & 7)) * 16);
    f32x4 s0 = (lds_ld<f32x4>(c0a) + lds_ld<f32x4>(c0a + 8192)) + (lds_ld<f32x4>(c0a + 16384) + lds_ld<f32x4>(c0a + 24576));
    f32x4 s1 = (lds_ld<f32x4>(c1a) + lds_ld<f32x4>(c1a + 8192)) + (lds_ld<f32x4>(c1a + 16384) + lds_ld<f32x4>(c1a + 24576));
    const float rs = lds_ld<float>(M_RS + 4 * tok);
    const v4u R = lds_ld<v4u>(H_U + tok * 144 + 16 * o);
    v4u res;
    UNROLL for (int j = 0; j < 4; ++j) {
      const u32 xj_ = xo[j], rj = R[j];
      const float bl = j < 2 ? s0[2 * (j & 1)] : s1[2 * (j & 1)], bh = j < 2 ? s0[2 * (j & 1) + 1] : s1[2 * (j & 1) + 1];
      res[j] = pk_bf16(__builtin_fmaf(bl, rs, bf_lo(xj_) + bf_lo(rj)), __builtin_fmaf(bh, rs, bf_hi(xj_) + bf_hi(rj)));
    }
    *(GAS v4u*)((GAS char*)PTR(kb, 1) + ooff) = res;
  }
}

extern "C" void kernel_launch(void* const* d_in, const int* in_sizes, int n_in, void* d_out, int out_size, void* d_ws, size_t ws_size,
                              hipStream_t stream) {
  fused_s4d_mlp<<<256, 512, 0, stream>>>((const u16*)d_in[0], (const u16*)d_in[1], (const u16*)d_in[2], (const u16*)d_in[3], (const u16*)d_in[4],
                                         (const u16*)d_in[5], (const u16*)d_in[6], (const u16*)d_in[8], (const u16*)d_in[9], (u16*)d_out,
                                         (char*)d_ws);
}
```

```cpp
#define PADAT 2
#define PADT 2000
#include <hip/hip_runtime.h>
#include <stdint.h>

typedef unsigned short u16;
typedef unsigned u32;
typedef unsigned long long u64;
typedef u32 v4u __attribute__((ext_vector_type(4)));
typedef u32 v2u __attribute__((ext_vector_type(2)));
typedef u32 v2u_a4 __attribute__((ext_vector_type(2), aligned(4)));
typedef int v4i __attribute__((ext_vector_type(4)));
typedef int v2i __attribute__((ext_vector_type(2)));
typedef int v16i __attribute__((ext_vector_type(16)));
typedef short v4s __attribute__((ext_vector_type(4)));
typedef float f32x2 __attribute__((ext_vector_type(2)));
typedef float f32x4 __attribute__((ext_vector_type(4)));
typedef float f32x16 __attribute__((ext_vector_type(16)));
typedef __bf16 bf16x2 __attribute__((ext_vector_type(2)));
typedef __bf16 bf16x8 __attribute__((ext_vector_type(8)));
typedef _Float16 f16x2 __attribute__((ext_vector_type(2)));
typedef _Float16 f16x8 __attribute__((ext_vector_type(8)));
typedef short s16x2 __attribute__((ext_vector_type(2)));
#define LAS __attribute__((address_space(3)))
#define GAS __attribute__((address_space(1)))
#if defined(__HIP_DEVICE_COMPILE__)
#define NOPK __attribute__((target("no-packed-fp32-ops")))
#else
#define NOPK
#endif
#define DI NOPK __device__ __forceinline__
#define RLX_AGENT __ATOMIC_RELAXED, __HIP_MEMORY_SCOPE_AGENT
#define SB() __builtin_amdgcn_sched_barrier(0)
#define PIN(x) asm volatile("" : "+v"(x))
#define UNROLL _Pragma("unroll")

#define DBG 0
#define NPRE 8
#define NM1 8
#define NM2 4
#define NPRO 20
#define KGRAN 22
#define SLP 1
#define PRIO 2
#define PSLP 2
#define NRL 4
#define NB 8
#define NFILL 8
#define YB0 4
#define YB1 12
#define HPRIO 3
#define EARLY 1
#define HWIN 5
#define XSLOT 1

#define ND0 ((32 - NPRO) / 4)
#define IS_D(K) ((K) >= 3 && (K) <= 18)
#define MIN_(A, B) ((A) < (B) ? (A) : (B))
#define RAW_BEFORE(K) (2 * ((K) - MIN_(K, ND0) - ((K) < 3 ? 0 : (K) > 19 ? 16 : (K) - 3)))
#define BEFORE(K) (NPRO + 2 * (K) + 2 * MIN_(K, ND0) + ((K) > KGRAN ? 4 : 0))
#define LASTPOS(T) ((T) <= 3 ? 8 * (T) + 7 : BEFORE(4 * (T) - 10) + 1)
#define GPOS (BEFORE(KGRAN + 1) - 1)
static_assert((NPRO & 3) == 0 && NPRO >= 20 && NPRO <= 32 && KGRAN >= 18 && KGRAN < 27, "load schedule");

constexpr int L = 2048, D = 512, NS = 128, F = 1792;
constexpr int WS_NONCE = 0;
constexpr int FL = 32;
constexpr int WS_CS = 16384;
constexpr int WS_ST = 32768;
constexpr int WS_FH = 65536;
constexpr int WS_FY = WS_FH + 131072;
constexpr int WS_XN = 524288;
constexpr int WS_Y = WS_XN + L * D * 2;
constexpr int WS_H = WS_Y + L * D * 2;
constexpr int WS_END = WS_H + L * F * 2;
constexpr int WS_DBG = 16 << 20;
constexpr int HB = 131072;
constexpr int H_U = HB;
constexpr int H_S = H_U + 9216;
constexpr int H_T = H_S + 17408;
constexpr int M_CF = H_T + 2688;
constexpr int M_HF = M_CF + 1024;
constexpr int M_TAG = M_HF + 1024;
constexpr int M_RS = M_TAG + 16;
constexpr int M_ST = M_RS + 256;
constexpr int LDS_BYTES = 163840;
constexpr int SPIN_CAP = 1 << 16;
constexpr int AUX_SC1 = 16;

DI float asf(u32 v) { return __builtin_bit_cast(float, v); }
DI u32 asu(float v) { return __builtin_bit_cast(u32, v); }
DI float bf_lo(u32 v) { return asf(v << 16); }
DI float bf_hi(u32 v) { return asf(v & 0xffff0000u); }
DI u32 pk_bf16(float lo, float hi) { f32x2 v = {lo, hi}; return __builtin_bit_cast(u32, __builtin_convertvector(v, bf16x2)); }
DI float rbf(float f) { return bf_lo(pk_bf16(f, f)); }
DI u32 pk_f16(float lo, float hi) { f32x2 v = {lo, hi}; return __builtin_bit_cast(u32, __builtin_convertvector(v, f16x2)); }
DI f32x16 mfma32(v4u a, v4u b, f32x16 c) { return __builtin_amdgcn_mfma_f32_32x32x16_bf16(__builtin_bit_cast(bf16x8, a), __builtin_bit_cast(bf16x8, b), c, 0, 0, 0); }
DI f32x4 mfma16h(v4u a, v4u b, f32x4 c) { return __builtin_amdgcn_mfma_f32_16x16x32_f16(__builtin_bit_cast(f16x8, a), __builtin_bit_cast(f16x8, b), c, 0, 0, 0); }
template <class T> DI T lds_ld(u32 a) { return *(const LAS T*)(uintptr_t)a; }
template <class T> DI void lds_st(u32 a, T v) { *(LAS T*)(uintptr_t)a = v; }
DI v2u lds_tr(u32 a) { return __builtin_bit_cast(v2u, __builtin_amdgcn_ds_read_tr16_b64_v4i16((LAS v4s*)(uintptr_t)a)); }
DI v4u cat(v2u a, v2u b) { v4u r = {a.x, a.y, b.x, b.y}; return r; }
DI bool all64(bool c) { return __builtin_amdgcn_ballot_w64(c) == ~0ull; }
DI u32 umin(u32 a, u32 b) { return a < b ? a : b; }
DI u32 umax(u32 a, u32 b) { return a > b ? a : b; }
template <int CTRL, int RM> DI float dpp_add(float v) { return v + asf(__builtin_amdgcn_update_dpp(0, asu(v), CTRL, RM, 0xf, true)); }
template <int CTRL, int RM> DI u32 dpp_max(u32 v) { return umax(v, (u32)__builtin_amdgcn_update_dpp(0, (int)v, CTRL, RM, 0xf, true)); }
#define BAR_L() asm volatile("s_waitcnt lgkmcnt(0)\n\ts_barrier" ::: "memory")
#define BAR_V(N) asm volatile("s_waitcnt vmcnt(%0) lgkmcnt(0)\n\ts_barrier" ::"n"(N) : "memory")
#define HBAR() do { SB(); BAR_L(); SB(); } while (0)
#define NBAR() do { SB(); asm volatile("s_barrier" ::: "memory"); SB(); } while (0)
#define WAIT_V(N) asm volatile("s_waitcnt vmcnt(%0)" ::"n"(N) : "memory")
#define FENCE() asm volatile("" ::: "memory")
DI v4i make_srd(const void* p, u32 bytes) { v4i s; s.x = (int)(uintptr_t)p; s.y = (int)((uintptr_t)p >> 32); s.z = (int)bytes; s.w = 0x00020000; return s; }
DI void rec_post(u32 rec, int w, u32 v) { FENCE(); *(volatile LAS u32*)(uintptr_t)(rec + 4 * w) = v; FENCE(); }
DI void rec_wait4(u32 rec, u32 v) {
#pragma unroll 1
  for (int i = 0; i < SPIN_CAP * 16; ++i) {
    const v4u f = *(volatile LAS v4u*)(uintptr_t)rec;
    if (all64(umin(umin(f.x, f.y), umin(f.z, f.w)) >= v)) break;
    __builtin_amdgcn_s_sleep(SLP);
  }
  FENCE();
}
DI void rec_wait1(u32 rec, int w, u32 v) {
#pragma unroll 1
  for (int i = 0; i < SPIN_CAP * 16; ++i) {
    const u32 f = *(volatile LAS u32*)(uintptr_t)(rec + 4 * w);
    if (all64(f >= v)) break;
    __builtin_amdgcn_s_sleep(SLP);
  }
  FENCE();
}
DI void poll_flag(const GAS u32* f, u32 tag) {
#pragma unroll 1
  for (int i = 0; i < SPIN_CAP; ++i) {
    const u32 v = __hip_atomic_load(f, RLX_AGENT);
    if (all64(v == tag)) break;
    __builtin_amdgcn_s_sleep(PSLP);
  }
}
#define AFTER_POLL() do { __builtin_amdgcn_fence(__ATOMIC_ACQUIRE, "wavefront"); FENCE(); } while (0)
DI u32 poll_granule(const GAS u64* g, u32 tag) {
  u64 v = 0;
#pragma unroll 1
  for (int i = 0; i < SPIN_CAP; ++i) {
    v = __hip_atomic_load(g, RLX_AGENT);
    if (all64((u32)(v >> 32) == tag)) break;
    __builtin_amdgcn_s_sleep(1);
  }
  return (u32)v;
}
DI float dot2(u32 a, u32 b, float c) { return __builtin_amdgcn_fdot2_f32_bf16(__builtin_bit_cast(bf16x2, a), __builtin_bit_cast(bf16x2, b), c, false); }
template <int V> struct ic { static constexpr int v = V; };
template <int I, int N, class Fn> DI void static_for(Fn&& f) { if constexpr (I < N) { f(ic<I>{}); static_for<I + 1, N>(f); } }
#define SFOR(VAR, N0, N1) static_for<N0, N1>([&](auto VAR##_) NOPK __attribute__((always_inline)) { constexpr int VAR = decltype(VAR##_)::v;
#define SEND });
struct cpx { float r, i; };
DI cpx cmul(cpx a, cpx b) { cpx c; c.r = __builtin_fmaf(a.r, b.r, -(a.i * b.i)); c.i = __builtin_fmaf(a.r, b.i, a.i * b.r); return c; }
DI cpx zpow(float a2, float th, float tl, float p) {
  const float mag = __builtin_amdgcn_exp2f(a2 * p);
  const float ph = __builtin_fmaf(tl, p, __builtin_amdgcn_fractf(th * p));
  cpx z; z.r = mag * __builtin_amdgcn_cosf(ph); z.i = mag * __builtin_amdgcn_sinf(ph); return z;
}
DI float exp_f(float x) {
  const float t = x * 1.44269502162933349609375f;
  const float e = __builtin_fmaf(x, 1.925963033500011079e-08f, __builtin_fmaf(x, 1.44269502162933349609375f, -t));
  const float r = __builtin_amdgcn_exp2f(t);
  const float c = __builtin_fmaf(r * e, 0.693147180559945f, r);
  return c == c ? c : r;
}

#define TS(i) do {} while (0)

#define HTS(i) do {} while (0)
#define CTS 0
NOPK __global__ __launch_bounds__(512, 2) void fused_s4d_mlp(const u16* a_x, const u16* a_lar, const u16* a_aim, const u16* a_cre, const u16* a_cim,
                                                             const u16* a_ldt, const u16* a_dsk, const u16* a_w1, const u16* a_w2, u16* a_out, char* a_ws) {
  __shared__ __attribute__((aligned(16))) char smem[LDS_BYTES];
  asm volatile("" ::"v"((u32)(uintptr_t)(LAS char*)smem));
  const u64 t_entry = __builtin_amdgcn_s_memrealtime();
#define PAD(ID) do { if (PADAT == (ID)) { for (int i_ = 0; i_ < 100000; ++i_) { if (__builtin_amdgcn_s_memrealtime() - t_entry >= PADT) break; __builtin_amdgcn_s_sleep(1); } } } while (0)
  PAD(1);
  typedef __attribute__((address_space(4))) const char* kptr;
  const kptr kas = (kptr)__builtin_amdgcn_kernarg_segment_ptr();
  const v16i ka = *(volatile __attribute__((address_space(4))) const v16i*)kas;
  const v4i kb = *(volatile __attribute__((address_space(4))) const v4i*)(kas + 64);
  const v2i kc = *(volatile __attribute__((address_space(4))) const v2i*)(kas + 80);
  SB();
#define ARGS_WAIT() SB()
#define PTR(V, I) ((char*)(((u64)(u32)(V)[2 * (I) + 1] << 32) | (u32)(V)[2 * (I)]))
#define RSRC(P, BYTES) __builtin_amdgcn_make_buffer_rsrc(P, 0, BYTES, 0x00020000)
  const int b = __builtin_amdgcn_workgroup_id_x(), xj = b & 7, r = b >> 3;
  const int tid = __builtin_amdgcn_workitem_id_x();
  const int wave = __builtin_amdgcn_readfirstlane(tid >> 6);
  const int l = tid & 63;
  const int tt = 4 * xj + (r >> 3), dt = r & 7;
  const v4u z4 = {0, 0, 0, 0};
  const u32 ooff = (64 * tt + (tid >> 3)) * 1024 + 128 * dt + 16 * (tid & 7);
  v4u xo;

  if (wave < 4) {
    const int hw = wave, rb = 32 * xj + r, c0 = 64 * xj + 2 * r, chl = hw >> 1, hm = hw & 1, ch = c0 + chl;
    const int g = l >> 4, i16 = l & 15;
    float p_lar = 0.f, p_aim = 0.f, p_cre = 0.f, p_cim = 0.f, p_cre2 = 0.f, p_cim2 = 0.f, p_ldt = 0.f, p_dsk = 0.f;
    u32 nonce;
    {
      u32 v1 = (ch * NS + 64 * hm + l) * 2, v2 = v1 ^ 128, v3 = ch * 2, v4 = 0;
      PIN(v1); PIN(v2); PIN(v3); PIN(v4);
      ARGS_WAIT();
      asm volatile("global_load_short_d16_hi %0, %9, %13\n\tglobal_load_short_d16_hi %6, %11, %17\n\tglobal_load_short_d16_hi %1, %9, %14\n\t"
                   "global_load_short_d16_hi %2, %9, %15\n\tglobal_load_short_d16_hi %3, %9, %16\n\tglobal_load_short_d16_hi %4, %10, %15\n\t"
                   "global_load_short_d16_hi %5, %10, %16\n\tglobal_load_short_d16_hi %7, %11, %18\n\tglobal_load_dword %8, %12, %19 sc1"
                   : "+&v"(p_lar), "+&v"(p_aim), "+&v"(p_cre), "+&v"(p_cim), "+&v"(p_cre2), "+&v"(p_cim2), "+&v"(p_ldt), "+&v"(p_dsk), "=&v"(nonce)
                   : "v"(v1), "v"(v2), "v"(v3), "v"(v4), "s"(PTR(ka, 1)), "s"(PTR(ka, 2)), "s"(PTR(ka, 3)), "s"(PTR(ka, 4)), "s"(PTR(ka, 5)),
                     "s"(PTR(ka, 6)), "s"(PTR(kc, 0)) : "memory");
    }
    char* const ws = PTR(kc, 0);
    const __amdgpu_buffer_rsrc_t rws = RSRC(ws, WS_END);
    TS(12);
    if (hw >= 2) lds_st<v4u>(M_CF + 16 * (tid - 128), z4);
    const u32 hrec = M_HF + 16 * l;
    const u32 tb = H_T + chl * 1344 + 16;
    u32 xa[17];
    {
      const int tau = 64 * hm + l, jtop = 2047 - 16 * tau;
      float fj = (float)jtop;
      PIN(fj);
      const bool ident = tau >= 112;
      const u32 sel = ident ? 0u : 0x1fffeu, base = ident ? tb + 2 * jtop : tb - 2 * 0x4280, stp = ident ? 2u : 0u;
      UNROLL for (int e = 0; e < 18; e += 2) {
        const u32 bits = pk_bf16(fj - (float)e, fj - (float)(e + 1));
        xa[e] = ((bits << 1) & sel) + (base - e * stp);
        if (e < 16) xa[e + 1] = ((bits >> 15) & sel) + (base - (e + 1) * stp);
      }
      UNROLL for (int e = 0; e < 17; ++e) PIN(xa[e]);
    }
    u32 sw[4];
    UNROLL for (int i = 0; i < 4; ++i) { sw[i] = H_S + hw * 4352 + 4 * l + 1088 * i; PIN(sw[i]); }
    u32 sr = H_S + hw * 4352 + i16 * 272 + 16 * g;
    u32 lnr = H_U + (i16 >> 2) * 1088 + (128 * hw + 16 * g + 4 * (l & 3)) * 2;
    u32 xno = WS_XN + rb * 8192 + (128 * hw + l) * 16;
    u32 uoff = WS_XN + (64 * hw + (l >> 1)) * 8192 + (c0 + (l & 1)) * 16;
    u32 ta0 = tb + 2 * (128 * (g >> 1) + 4 * (g & 1) + 8 * i16), ta1 = tb + 2 * (g < 2 ? 256 + 64 * g + 4 * i16 : 384 + 64 * (g - 2) + 2 * i16);
    u32 ta1b = ta1 + (g < 2 ? 4 : 64), ta2 = g < 3 ? tb + 2 * (512 + 64 * g + i16) : tb - 2;
    PIN(sr); PIN(lnr); PIN(xno); PIN(uoff); PIN(ta0); PIN(ta1); PIN(ta1b); PIN(ta2);
    TS(10);
    asm volatile("s_waitcnt vmcnt(0)" : "+v"(p_lar), "+v"(p_aim), "+v"(p_cre), "+v"(p_cim), "+v"(p_cre2), "+v"(p_cim2), "+v"(p_ldt), "+v"(p_dsk), "+v"(nonce)::"memory");
    TS(11);
    const u32 tag = __builtin_amdgcn_readfirstlane(nonce) * 0x9E3779B1u + 0x7F4A7C15u;
    if (tid == 0) lds_st<u32>(M_TAG, tag);
    const float dtv = rbf(exp_f(p_ldt));
    const float alpha = -rbf(exp_f(p_lar)) * dtv, beta = p_aim * dtv;
    const float a2 = alpha * 1.44269504088896341f;
    const float th = beta * 0.15915494309189535f;
    const float tl = __builtin_fmaf(beta, 6.42063831672e-09f, __builtin_fmaf(beta, 0.15915494309189535f, -th));
    u32 mx = umax(umax(asu(p_cre) & 0x7fffffffu, asu(p_cim) & 0x7fffffffu), umax(asu(p_cre2) & 0x7fffffffu, asu(p_cim2) & 0x7fffffffu));
    mx = dpp_max<0xB1, 0xf>(mx); mx = dpp_max<0x4E, 0xf>(mx); mx = dpp_max<0x124, 0xf>(mx); mx = dpp_max<0x128, 0xf>(mx);
    mx = dpp_max<0x142, 0xa>(mx); mx = dpp_max<0x143, 0xc>(mx);
    int ex = (int)(__builtin_amdgcn_readlane(mx, 63) >> 23) + 1;
    ex = ex < 32 ? 32 : (ex > 230 ? 230 : ex);
    const float cscale = asf((u32)(254 + 14 - ex) << 23);
    const float yscale = asf((u32)(ex - 17) << 23) * dtv;
    cpx z8c = zpow(a2, th, tl, 8.f);
    z8c.i = -z8c.i;
    u32 tgb[4];
    UNROLL for (int j = 0; j < 4; ++j) tgb[j] = ((tag >> (6 * j)) & 7u) | (((tag >> (6 * j + 3)) & 7u) << 16);
    u32 an[57];
#define KEEP(I, Z) do { const cpx z_ = (Z); an[I] = pk_f16(z_.r, z_.i); PIN(an[I]); } while (0)
    cpx e0[16], e1[16];
    cpx y; y.r = p_cre * cscale; y.i = -p_cim * cscale;
    UNROLL for (int i = 0; i < YB0; ++i) { KEEP(i, y); y = cmul(y, z8c); }
    HTS(16);
    HBAR();
    {
      const u32 stv = lds_ld<u32>(M_ST + 4 * (l & 7));
      v4u o[2];
      UNROLL for (int k = 0; k < 2; ++k) o[k] = cat(lds_tr(lnr + 128 * k), lds_tr(lnr + 128 * k + 4 * 1088));
      SB();
      UNROLL for (int i = YB0; i < YB0 + 4; ++i) { KEEP(i, y); y = cmul(y, z8c); }
      SB();
      UNROLL for (int k = 0; k < 2; ++k) {
        UNROLL for (int j = 0; j < 4; ++j) o[k][j] = (o[k][j] & ~0x00070007u) | tgb[j];
        __builtin_amdgcn_raw_buffer_store_b128(o[k], rws, xno + 1024 * k, 0, AUX_SC1);
      }
      if (tid < 8) __hip_atomic_store((GAS u64*)(ws + WS_ST) + 16 * rb + tid, ((u64)tag << 32) | stv, RLX_AGENT);
      HTS(1);
      UNROLL for (int i = YB0 + 4; i < YB1; ++i) { KEEP(i, y); if (i < 15) y = cmul(y, z8c); }
    }
    HTS(17);
    NBAR();
    UNROLL for (int i = YB1; i < 16; ++i) { KEEP(i, y); if (i < 15) y = cmul(y, z8c); }
    const cpx z1 = zpow(a2, th, tl, 1.f);
    e0[0].r = 32768.f; e0[0].i = 0.f;
    e0[1].r = z1.r * 32768.f; e0[1].i = z1.i * 32768.f;
    UNROLL for (int i = 2; i < 8; ++i) e0[i] = cmul(e0[i - 1], z1);
    UNROLL for (int i = 0; i < 8; ++i) KEEP(16 + i, e0[i]);
    HTS(18);
    NBAR();
    const cpx z128 = zpow(a2, th, tl, 128.f);
    e0[8].r = z128.r * 32768.f; e0[8].i = z128.i * 32768.f;
    UNROLL for (int i = 9; i < 16; ++i) e0[i] = cmul(e0[i - 8], z128);
    UNROLL for (int i = 8; i < 16; ++i) KEEP(16 + i, e0[i]);
    const cpx z256 = cmul(z128, z128), z512 = cmul(z256, z256);
    UNROLL for (int i = 0; i < 2; ++i) { e1[i] = cmul(e0[8 * (i >> 2) + 2 * (i & 3)], z256); KEEP(32 + i, e1[i]); }
    HTS(19);
    NBAR();
    UNROLL for (int i = 2; i < 8; ++i) { e1[i] = cmul(e0[8 * (i >> 2) + 2 * (i & 3)], z256); KEEP(32 + i, e1[i]); }
    UNROLL for (int i = 0; i < 4; ++i) { e1[8 + i] = cmul(e0[8 * (i >> 1) + 4 * (i & 1)], z512); KEEP(40 + i, e1[8 + i]); }
    UNROLL for (int i = 0; i < 4; ++i) { e1[12 + i] = cmul(e1[4 * (i >> 1) + 2 * (i & 1)], z512); KEEP(44 + i, e1[12 + i]); }
    HTS(20);
    NBAR();
    {
      const cpx z1024 = zpow(a2, th, tl, 1024.f);
      cpx e2;
      e2.r = z1024.r * 32768.f; e2.i = z1024.i * 32768.f;
      KEEP(48, e2); KEEP(49, cmul(e0[8], z1024)); KEEP(50, cmul(e1[0], z1024)); KEEP(51, cmul(e1[4], z1024));
      KEEP(52, cmul(e1[8], z1024)); KEEP(53, cmul(e1[10], z1024)); KEEP(54, cmul(e1[12], z1024)); KEEP(55, cmul(e1[14], z1024));
      KEEP(56, cmul(e2, z1024));
    }
    v4u uv[2];
    UNROLL for (int i = 0; i < 2; ++i) uv[i] = __builtin_amdgcn_raw_buffer_load_b128(rws, uoff + i * 32 * 8192, 0, AUX_SC1);
    HTS(21);
    NBAR();
    __builtin_amdgcn_s_setprio(HPRIO);
    const u32 kx = H_S + (hw | 1) * 4352 + 16 * l;
    f32x4 kacc[3];
    {
      v4u yb[4], xf[3][4];
      if (EARLY == 0) NBAR();
      UNROLL for (int i = 0; i < 16; ++i) lds_st<u32>(sw[i >> 2] + 272 * (i & 3), an[i]);
      UNROLL for (int s = 0; s < 4; ++s) yb[s] = lds_ld<v4u>(sr + 64 * s);
      UNROLL for (int t = 0; t < 3; ++t) {
        if (EARLY == t + 1) NBAR();
        UNROLL for (int i = 0; i < (t < 2 ? 16 : 9); ++i) lds_st<u32>(sw[i >> 2] + 272 * (i & 3), an[16 + 16 * t + i]);
        UNROLL for (int s = 0; s < 4; ++s) xf[t][s] = lds_ld<v4u>(sr + 64 * s);
        if (t > 0) {
          SB();
          kacc[t - 1] = f32x4{0.f, 0.f, 0.f, 0.f};
          UNROLL for (int s = 0; s < 4; ++s) kacc[t - 1] = mfma16h(xf[t - 1][s], yb[s], kacc[t - 1]);
          SB();
        }
      }
      kacc[2] = f32x4{0.f, 0.f, 0.f, 0.f};
      UNROLL for (int s = 0; s < 4; ++s) kacc[2] = mfma16h(xf[2][s], yb[s], kacc[2]);
    }
    TS(24);
#define U_PUT() do { UNROLL for (int i = 0; i < 2; ++i) { \
      _Pragma("unroll 1") for (int it = 0; it < SPIN_CAP; ++it) { \
        UNROLL for (int j = 0; j < 4; ++j) uv[i][j] ^= tgb[j]; \
        if ((((uv[i].x | uv[i].y) | (uv[i].z | uv[i].w)) & 0x00070007u) == 0) break; \
        __builtin_amdgcn_s_sleep(PSLP); \
        FENCE(); \
        uv[i] = __builtin_amdgcn_raw_buffer_load_b128(rws, uoff + i * 32 * 8192, 0, AUX_SC1); \
      } \
      lds_st<v4u>(uw + 512 * i, uv[i]); } } while (0)
    const u32 uw = H_U + (l & 1) * 4608 + 512 + (64 * hw + (l >> 1)) * 16;
    if (hm) {
      UNROLL for (int i = 0; i < 3; ++i) lds_st<f32x4>(kx + 1024 * i, kacc[i]);
      rec_post(hrec, hw, 2);
      if (l < 32) lds_st<v4u>(H_U + chl * 4608 + 16 * l, z4);
      rec_wait1(hrec, hw ^ 1, 2);
    } else {
      rec_wait1(hrec, hw ^ 1, 2);
      u32 kp[3][2];
      UNROLL for (int i = 0; i < 3; ++i) {
        const f32x4 v = (kacc[i] + lds_ld<f32x4>(kx + 1024 * i)) * 4.76837158203125e-07f;
        kp[i][0] = pk_f16(v[0], v[1]); kp[i][1] = pk_f16(v[2], v[3]);
      }
      lds_st<v2u>(ta0, v2u{kp[0][0], kp[0][1]});
      lds_st<u32>(ta1, kp[1][0]); lds_st<u32>(ta1b, kp[1][1]);
      lds_st<u16>(ta2, (u16)(g < 3 ? kp[2][0] : 0u));
      if (g < 2) { lds_st<u16>(ta2 + 32, (u16)(kp[2][0] >> 16)); lds_st<u16>(ta2 + 64, (u16)kp[2][1]); lds_st<u16>(ta2 + 96, (u16)(kp[2][1] >> 16)); }
      rec_post(hrec, hw, 2);
    }
    TS(3);
    const u32 eb = H_S + chl * 8704;
    {
      u32 kv[17];
      UNROLL for (int e = 0; e < 17; ++e) kv[e] = lds_ld<u16>(xa[e]);
      SB();
      U_PUT();
      SB();
      v4u ea0, ea1, eb0, eb1;
      UNROLL for (int e = 0; e < 4; ++e) {
        ea0[e] = kv[2 * e] | (kv[2 * e + 1] << 16); ea1[e] = kv[2 * e + 8] | (kv[2 * e + 9] << 16);
        eb0[e] = kv[2 * e + 1] | (kv[2 * e + 2] << 16); eb1[e] = kv[2 * e + 9] | (kv[2 * e + 10] << 16);
      }
      const u32 a = eb + 32 * (64 * hm + l);
      lds_st<v4u>(a, ea0); lds_st<v4u>(a + 16, ea1); lds_st<v4u>(a + 4160, eb0); lds_st<v4u>(a + 4176, eb1);
      if (l < 4 && hm == 0) lds_st<v4u>(eb + 4096 + (l >> 1) * 4160 + 16 * (l & 1), z4);
    }
    rec_post(hrec, hw, 4);
    u32 fa4[4];
    {
      const int x0 = 2047 - 16 * (1 + 8 * hm) - i16 + 8 * g;
      const u32 a = eb + ((i16 & 1) ? 0 : 4160) + 4 * (x0 >> 1) - 512 * 7 - 192;
      UNROLL for (int i = 0; i < 4; ++i) { fa4[i] = a + 1024 * (3 - i); PIN(fa4[i]); }
    }
#define FA_LD(W, MI) cat(*(const LAS v2u_a4*)(uintptr_t)(fa4[(W) >> 1] + 512 * (1 - ((W) & 1)) + 192 - 64 * (MI)), \
                         *(const LAS v2u_a4*)(uintptr_t)(fa4[(W) >> 1] + 512 * (1 - ((W) & 1)) + 192 - 64 * (MI) + 8))
    v4u FA[4][8], G[4][8];
    TS(4);
    rec_wait4(hrec, 4);
    if (b == 0 && tid == 0) __hip_atomic_store((GAS u32*)(ws + WS_NONCE), tag, RLX_AGENT);
    TS(5);
    f32x4 acc[8];
    UNROLL for (int i = 0; i < 8; ++i) acc[i] = f32x4{0.f, 0.f, 0.f, 0.f};
    const u32 ub = H_U + chl * 4608 + 512;
    v2u uu[4];
    u32 pf;
    {
      const u32 a = ub + 32 * i16 + 16 * g - 32 * (1 + 8 * hm) - 192;
      G[0][0] = lds_ld<v4u>(a + 192);
      UNROLL for (int w = 0; w < 8; ++w) FA[0][w] = FA_LD(w, 0);
      UNROLL for (int v = 1; v < 8; ++v) G[0][v] = lds_ld<v4u>(a + 512 * v + 192);
      UNROLL for (int mi = 0; mi < 4; ++mi) {
        UNROLL for (int v = 0; v < 8; ++v) {
          SB();
          UNROLL for (int w = 0; v + w < 8; ++w) {
            acc[v + w] = mfma16h(FA[mi][w], G[mi][v], acc[v + w]);
            if (w == 0 && mi < 3) { SB(); G[mi + 1][v] = lds_ld<v4u>(a + 512 * v + 192 - 64 * (mi + 1)); SB(); }
            if (mi < 3 && v < 4 && (w == 1 || w == 3)) { SB(); FA[mi + 1][2 * v + (w >> 1)] = FA_LD(2 * v + (w >> 1), mi + 1); SB(); }
            if (mi == 3 && w == 0 && v < 4) { SB(); uu[v] = lds_ld<v2u>(ub + 2 * (256 * (4 * hm + v) + 16 * i16 + 4 * g)); SB(); }
            if (mi == 3 && w == 0 && v == 4) { SB(); pf = *(volatile LAS u32*)(uintptr_t)(hrec + 4 * (hw ^ 1)); SB(); }
          }
          if (mi == 2 && v == 4) rec_post(hrec, hw, 5);
        }
      }
    }
    TS(6);
    {
      const u32 xw = H_S + hw * 4352 + 16 * l, xrd = H_S + (hw ^ 1) * 4352 + 16 * l;
      if (!all64(pf >= 5)) rec_wait1(hrec, hw ^ 1, 5);
      FENCE();
      UNROLL for (int i = 0; i < 4; ++i) { const f32x4 lo = acc[i], hi = acc[4 + i]; lds_st<f32x4>(xw + 1024 * i, hm ? lo : hi); acc[i] = hm ? hi : lo; }
      rec_post(hrec, hw, 6);
      const float dsv = p_dsk * (1.0f / 1024.0f);
      u32 yo = WS_Y + ch * 4096 + hm * 2048 + 16 * l;
      PIN(yo);
      UNROLL for (int i = 0; i < 4; ++i) {
        const u32 ux = uu[i].x, uy = uu[i].y;
        const f16x2 u01 = __builtin_bit_cast(f16x2, ux), u23 = __builtin_bit_cast(f16x2, uy);
        acc[i][0] = __builtin_fmaf((float)u01.x, dsv, acc[i][0] * yscale); acc[i][1] = __builtin_fmaf((float)u01.y, dsv, acc[i][1] * yscale);
        acc[i][2] = __builtin_fmaf((float)u23.x, dsv, acc[i][2] * yscale); acc[i][3] = __builtin_fmaf((float)u23.y, dsv, acc[i][3] * yscale);
        PIN(acc[i]);
      }
      rec_wait1(hrec, hw ^ 1, 6);
      u32 o[4][2];
      UNROLL for (int i = 0; i < 4; ++i) {
        const f32x4 y = lds_ld<f32x4>(xrd + 1024 * i);
        o[i][0] = pk_bf16(__builtin_fmaf(y[0], yscale, acc[i][0]), __builtin_fmaf(y[1], yscale, acc[i][1]));
        o[i][1] = pk_bf16(__builtin_fmaf(y[2], yscale, acc[i][2]), __builtin_fmaf(y[3], yscale, acc[i][3]));
      }
      UNROLL for (int i = 0; i < 4; i += 2) {
        const auto s0 = __builtin_amdgcn_permlane16_swap(o[i][0], o[i + 1][0], false, false);
        const auto s1 = __builtin_amdgcn_permlane16_swap(o[i][1], o[i + 1][1], false, false);
        const v4u v = {s0[0], s1[0], s0[1], s1[1]};
        __builtin_amdgcn_raw_buffer_store_b128(v, rws, yo + (i >> 1) * 1024, 0, AUX_SC1);
      }
      rec_post(hrec, hw, 7);
      TS(2);
      WAIT_V(0);
      if (l == 0) __hip_atomic_store((GAS u32*)(ws + WS_FY) + FL * (4 * (c0 >> 1) + hw), tag, RLX_AGENT);
    }
    TS(7);
    xo = __builtin_amdgcn_raw_buffer_load_b128(RSRC(PTR(ka, 0), L * D * 2), ooff, 0, 0);
    if (hw == 0) lds_st<float>(M_RS + 4 * l, __builtin_amdgcn_rcpf(bf_hi(poll_granule((const GAS u64*)(ws + WS_ST) + 16 * (8 * tt + (l >> 3)) + (l & 7), tag))));
    poll_flag((const GAS u32*)(ws + WS_FY) + FL * (4 * (32 * dt + 8 * hw + (i16 >> 1)) + 2 * (l & 1) + (tt >> 4)), tag);
    AFTER_POLL();
    TS(8);
    {
      v4u yv[2];
      const int pc = l & 7;
      UNROLL for (int i = 0; i < 2; ++i)
        yv[i] = __builtin_amdgcn_raw_buffer_load_b128(rws, WS_Y + (64 * dt + 16 * hw + 8 * i + (l >> 3)) * 4096 + (tt >> 3) * 1024 +
                                                     (2 * (pc >> 2) + ((tt >> 2) & 1)) * 256 + (4 * (tt & 3) + (pc & 3)) * 16, 0, AUX_SC1);
      rec_wait4(hrec, 7);
      const u32 yst = H_S + hw * 4352;
      UNROLL for (int i = 0; i < 2; ++i) lds_st<v4u>(yst + (8 * i + (l >> 3)) * 192 + 32 * (pc & 3) + 16 * (pc >> 2), yv[i]);
      const u32 a = yst + (i16 >> 2) * 192 + 32 * g + 8 * (l & 3);
      const v4u y0 = cat(lds_tr(a), lds_tr(a + 4 * 192)), y1 = cat(lds_tr(a + 8 * 192), lds_tr(a + 12 * 192));
      lds_st<v4u>(H_U + l * 144 + 32 * hw, y0);
      lds_st<v4u>(H_U + l * 144 + 32 * hw + 16, y1);
    }
    TS(9);
  } else {
    __builtin_amdgcn_s_setprio(PRIO);
    const int w = wave - 4, wf = w & 1, wt = w >> 1;
    const int h = l >> 5, r32 = l & 31, blk = (l >> 4) & 1, q = (l & 15) >> 2, p = l & 3;
    const int mt = 4 * (xj >> 1) + (r & 3), nt = 7 * (xj & 1) + (r >> 2);
    u32 v_x[4], v_w1[4], v_l = 16 * l;
    UNROLL for (int i = 0; i < 4; ++i) {
      v_x[i] = (l >> 3) * 1024 + (((l & 7) ^ (l >> 4) ^ (4 * (i & 1))) * 16) + i * 7168;
      v_w1[i] = (l >> 4) * 3584 + (((l & 15) ^ ((l >> 4) << 2)) * 16) + i * (4 * 3584 - 1024);
      PIN(v_x[i]); PIN(v_w1[i]);
    }
    u32 s_x = (128 * mt + 32 * w) * 1024, s_w1 = (16 * w) * 3584 + 256 * nt, s_r = (256 * xj + 8 * r + 2 * w) * 1024;
    const int iu = 4 * (xj >> 1) + r - 28, ii = 14 * (xj & 1) + (iu < 14 ? iu : 0);
    asm volatile("" : "+s"(s_x), "+s"(s_w1), "+s"(s_r));
    PIN(v_l);
    ARGS_WAIT();
    TS(25);
    char* const ws = PTR(kc, 0);
    const v4i sx = {ka[0], ka[1], L * D * 2, 0x00020000}, sw1 = {ka[14], ka[15], D * F * 2, 0x00020000}, sws = {kc[0], kc[1], WS_END, 0x00020000};
    v4u xr[2];
    asm volatile("buffer_load_dwordx4 %0, %2, %3, %4 offen\n\tbuffer_load_dwordx4 %1, %2, %3, %4 offen offset:1024"
                 : "=&v"(xr[0]), "=&v"(xr[1]) : "v"(v_l), "s"(sx), "s"(s_r) : "memory");
    u32 so;
#define SET_M0(V) asm volatile("s_mov_b32 m0, %1" : "+s"(so) : "s"(V) : "memory")
#define GLDS(VO, SRD, I) asm volatile("buffer_load_dwordx4 %0, %1, %2 offen offset:%3 lds" ::"v"(VO), "s"(SRD), "s"(so), "n"((I) * 1024) : "memory")
#define SETG(N) do { const int G_ = (N) >> 2, T_ = G_ >> 1; \
        if (G_ & 1) { so = s_w1 + T_ * (64 * 3584); SET_M0((T_ & 3) * 32768 + 16384 + w * 4096); } \
        else { so = s_x + T_ * 128; SET_M0((T_ & 3) * 32768 + w * 4096); } } while (0)
#define GL(N) do { if (((N) >> 2) & 1) GLDS(v_w1[(N) & 3], sw1, (N) & 3); else GLDS(v_x[(N) & 3], sx, (N) & 3); } while (0)
#define GLRUN(N0, N1) do { UNROLL for (int n_ = (N0); n_ < (N1); ++n_) { if ((n_ & 3) == 0) { SETG(n_); asm volatile("s_nop 0"); } GL(n_); } } while (0)
    v4u wv[16];
    const u32 v_c = (l >> 3) * 3584 + (l & 7) * 16;
#define CSRUN(N0, N1) do { UNROLL for (int i_ = (N0); i_ < (N1); ++i_) { const u32 sof_ = (128 * w + 8 * i_) * 3584 + 128 * ii; \
        asm volatile("buffer_load_dwordx4 %0, %1, %2, %3 offen" : "=&v"(wv[i_]) : "v"(v_c), "s"(sw1), "s"(sof_) : "memory"); } } while (0)
    if (r < 28) GLRUN(0, NPRE); else CSRUN(0, NPRE);
    const __amdgpu_buffer_rsrc_t rws = RSRC(ws, WS_END), rw2 = RSRC(PTR(kb, 0), F * D * 2);
    const u32 v_w2 = (l >> 3) * 1024 + (l & 7) * 16;
    v4u wr[56];
#define RAW(I) wr[I] = __builtin_amdgcn_raw_buffer_load_b128(rw2, v_w2, (448 * w + 8 * (I)) * 1024 + 128 * dt, 0)
    u32 ab[2][4], aa[2][2];
    {
      const int row = 64 * wt + r32, z = (row >> 1) & 7;
      UNROLL for (int s = 0; s < 4; ++s) { ab[0][s] = row * 128 + (((2 * s + h) ^ z) * 16); ab[1][s] = ab[0][s] + 65536; PIN(ab[0][s]); PIN(ab[1][s]); }
      UNROLL for (int f2 = 0; f2 < 2; ++f2) {
        aa[0][f2] = 16384 + (8 * h + q) * 256 + (((8 * wf + 4 * f2 + 2 * blk + (p >> 1)) ^ (q << 2)) * 16) + 8 * (p & 1);
        aa[1][f2] = aa[0][f2] + 65536; PIN(aa[0][f2]); PIN(aa[1][f2]);
      }
    }
    u32 wa[2], lnw = H_U + 2 * w * 1088 + 16 * l;
    wa[0] = w * 8192 + (l >> 3) * 128 + (((l & 7) ^ (((l >> 4) & 1) << 2)) * 16); wa[1] = wa[0] + 65536;
    PIN(wa[0]); PIN(wa[1]); PIN(lnw);
#define W2_ST(I) lds_st<v4u>(wa[((I) >> 4) & 1] + (((I) >> 3) & 1) * 32768 + ((I) & 7) * 1024, wr[I])
    TS(9);
    asm volatile("s_waitcnt vmcnt(%2)" : "+v"(xr[0]), "+v"(xr[1]) : "n"(NPRE) : "memory");
    TS(12);
    {
      float s[2], qq[2], d[2][8], m[2], var[2];
      UNROLL for (int k = 0; k < 2; ++k) {
        s[k] = 0.f; qq[k] = 0.f;
        UNROLL for (int j = 0; j < 4; ++j) { const u32 u = xr[k][j]; s[k] = dot2(u, 0x3f803f80u, s[k]); qq[k] = dot2(u, u, qq[k]); }
      }
#define RED4(C, RM) do { s[0] = dpp_add<C, RM>(s[0]); qq[0] = dpp_add<C, RM>(qq[0]); s[1] = dpp_add<C, RM>(s[1]); qq[1] = dpp_add<C, RM>(qq[1]); } while (0)
      RED4(0xB1, 0xf); RED4(0x4E, 0xf); RED4(0x124, 0xf); RED4(0x128, 0xf); RED4(0x142, 0xa); RED4(0x143, 0xc);
      SB();
      if (r < 28) GLRUN(NPRE, NPRE + NM1); else CSRUN(NPRE, 16);
      SB();
      float e1[2];
      bool redo = false;
      UNROLL for (int k = 0; k < 2; ++k) {
        e1[k] = asf(__builtin_amdgcn_readlane(asu(s[k]), 63)) * (1.0f / D);
        const float e2 = asf(__builtin_amdgcn_readlane(asu(qq[k]), 63)) * (1.0f / D);
        var[k] = __builtin_fmaf(-e1[k], e1[k], e2);
        redo |= !(var[k] * 16.f >= e2);
        m[k] = rbf(e1[k]);
        UNROLL for (int j = 0; j < 4; ++j) { const u32 u = xr[k][j]; d[k][2 * j] = bf_lo(u) - m[k]; d[k][2 * j + 1] = bf_hi(u) - m[k]; }
      }
      if (__builtin_amdgcn_readfirstlane(redo)) {
        UNROLL for (int k = 0; k < 2; ++k) {
          s[k] = 0.f; qq[k] = 0.f;
          UNROLL for (int j = 0; j < 8; ++j) { s[k] += d[k][j]; qq[k] = __builtin_fmaf(d[k][j], d[k][j], qq[k]); }
        }
        RED4(0xB1, 0xf); RED4(0x4E, 0xf); RED4(0x124, 0xf); RED4(0x128, 0xf); RED4(0x142, 0xa); RED4(0x143, 0xc);
        UNROLL for (int k = 0; k < 2; ++k) {
          const float c1 = asf(__builtin_amdgcn_readlane(asu(s[k]), 63)) * (1.0f / D), c2 = asf(__builtin_amdgcn_readlane(asu(qq[k]), 63)) * (1.0f / D);
          var[k] = __builtin_fmaxf(__builtin_fmaf(-c1, c1, c2), 0.f);
        }
      }
      u32 pay[2];
      UNROLL for (int k = 0; k < 2; ++k) {
        const float sd = rbf(__builtin_amdgcn_sqrtf(rbf(rbf(var[k]) + 1e-5f)));
        const float rs = 1024.0f * __builtin_amdgcn_rcpf(sd);
        v4u o;
        UNROLL for (int j = 0; j < 4; ++j) {
          const u32 dd = pk_bf16(d[k][2 * j], d[k][2 * j + 1]);
          const u32 n = pk_bf16(bf_lo(dd) * rs, bf_hi(dd) * rs);
          o[j] = pk_f16(bf_lo(n), bf_hi(n));
        }
        lds_st<v4u>(lnw + k * 1088, o);
        pay[k] = (asu(m[k]) >> 16) | (asu(sd) & 0xffff0000u);
        if (k == 0) { SB(); if (r < 28) GLRUN(NPRE + NM1, NPRE + NM1 + NM2); SB(); }
      }
      lds_st<v2u>(M_ST + 8 * w, v2u{pay[0], pay[1]});
    }
    SB();
    TS(13);
    u32 tag;
    constexpr int NRAW0 = RAW_BEFORE(28) + NRL;
    constexpr int NJOIN = NRAW0 + NB;
    if (r < 28) {
      GLRUN(NPRE + NM1 + NM2, NPRO);
      const u32 crec = M_CF + 16 * l;
      f32x16 acc[2][2];
      UNROLL for (int i = 0; i < 2; ++i) UNROLL for (int j = 0; j < 2; ++j) UNROLL for (int e = 0; e < 16; ++e) acc[i][j][e] = 0.f;
      v4u fb[2][4][2];
      v2u fl[2][4][2], fh[2][4][2];
      v2u gm[2], gc[2];
#define RD(T, s_, j_) do { const int hi_ = ((T) >> 1) & 1, o_ = ((T) & 1) * 32768, u_ = (T) & 1; \
        if ((j_) == 0) fb[u_][s_][0] = lds_ld<v4u>(ab[hi_][s_] + o_); \
        if ((j_) == 1) fl[u_][s_][0] = lds_tr(aa[hi_][0] + o_ + (s_) * 4096); \
        if ((j_) == 2) fh[u_][s_][0] = lds_tr(aa[hi_][0] + o_ + (s_) * 4096 + 1024); \
        if ((j_) == 3) fb[u_][s_][1] = lds_ld<v4u>(ab[hi_][s_] + o_ + 4096); \
        if ((j_) == 4) fl[u_][s_][1] = lds_tr(aa[hi_][1] + o_ + (s_) * 4096); \
        if ((j_) == 5) fh[u_][s_][1] = lds_tr(aa[hi_][1] + o_ + (s_) * 4096 + 1024); } while (0)
#define RDN(T, J0, N) do { UNROLL for (int j2_ = (J0); j2_ < (J0) + (N); ++j2_) { \
        if ((T) < 7) RD(T, j2_ / 6, j2_ % 6); \
        else if (j2_ < 12) RD(T, j2_ / 3, j2_ % 3); else if (j2_ < 16) RD(T, (j2_ - 12) & 3, 3); else RD(T, ((j2_ - 16) >> 1) & 3, 4 + (j2_ & 1)); } } while (0)
#define MM(T, S, I, J) acc[I][J] = mfma32(cat(fl[(T) & 1][S][I], fh[(T) & 1][S][I]), fb[(T) & 1][S][J], acc[I][J])
#define SLOT(K, J) do { if ((K) < ND0) { GL(NPRO + 4 * (K) + 2 * (J)); GL(NPRO + 4 * (K) + 2 * (J) + 1); } \
        else if (IS_D(K)) GL(26 + 2 * (K) + (J)); else RAW(RAW_BEFORE(K) + (J)); } while (0)
#define NEXTG(K) do { if ((K) < ND0) SETG(NPRO + 4 * (K)); else if (IS_D(K) && ((K) & 1)) SETG(26 + 2 * (K)); } while (0)
      NEXTG(0);
      u32 v_g = r32 * 8, v_m = (r32 >> 3) * 128 + (r32 & 7) * 8, ho[2];
      UNROLL for (int j = 0; j < 2; ++j) { ho[j] = WS_H + ((4 * mt + 2 * wt + j) * 112 + 8 * nt + 4 * wf) * 1024 + 16 * l; PIN(ho[j]); }
      PIN(v_g); PIN(v_m);
      TS(16);
      PAD(2);
      BAR_V(NPRO - 8);
      TS(1);
      RDN(0, 0, 10);
      v4u ca[2], cb[2], cf;
      u32 tagv;
      SFOR(k, 0, 28)
        constexpr int t = k >> 2, s = k & 3;
        SB();
        MM(t, s, 0, 0);
        SB();
        if (s == 3) {
          if (t < 5) BAR_V(BEFORE(k) - LASTPOS(t + 1) - 1);
          if (t == 5) BAR_V(BEFORE(k) - LASTPOS(7) - 1);
          if (CTS || t == 3 || t == 5 || t == 6) TS(17 + t);
          RDN(t + 1, 0, 2);
        }
        SLOT(k, 0);
        SB();
        MM(t, s, 0, 1);
        SB();
        if (s == 3) RDN(t + 1, 2, 4);
        if (s == 0) RDN(t, 10, 4);
        if (s == 1) RDN(t, 17, 4);
        if (k == 2) tagv = lds_ld<u32>(M_TAG);
        SB();
        MM(t, s, 1, 0);
        SB();
        SLOT(k, 1);
        SB();
        MM(t, s, 1, 1);
        SB();
        if (s == 3) RDN(t + 1, 6, 4);
        if (s == 0) RDN(t, 14, 3);
        if (s == 1) RDN(t, 21, 3);
        if (k == KGRAN) {
          const u32 so_m = WS_ST + (16 * mt + 8 * wt) * 128, so_c = WS_CS + (128 * nt + 64 * wf) * 8;
          asm volatile("buffer_load_dwordx2 %0, %8, %5, %6 offen sc1\n\tbuffer_load_dwordx2 %1, %8, %5, %6 offen offset:512 sc1\n\t"
                       "buffer_load_dwordx2 %2, %4, %5, %7 offen sc1\n\tbuffer_load_dwordx2 %3, %4, %5, %7 offen offset:256 sc1"
                       : "=&v"(gm[0]), "=&v"(gm[1]), "=&v"(gc[0]), "=&v"(gc[1]) : "v"(v_g), "s"(sws), "s"(so_m), "s"(so_c), "v"(v_m) : "memory");
        }
        if (k == 6) tag = __builtin_amdgcn_readfirstlane(tagv);
        NEXTG(k + 1);
      SEND
      v4u ho_;
#define CVT(I, J, S, E) do { const s16x2 v_ = __builtin_bit_cast(s16x2, pk_bf16(acc[I][J][8 * (S) + 2 * (E)], acc[I][J][8 * (S) + 2 * (E) + 1])); \
        const s16x2 z_ = {0, 0}; ho_[E] = __builtin_bit_cast(u32, __builtin_elementwise_max(v_, z_)); } while (0)
#define HST(I, J, S) __builtin_amdgcn_raw_buffer_store_b128(ho_, rws, ho[J] + (2 * (I) + (S)) * 1024, 0, AUX_SC1)
      UNROLL for (int m = 0; m < 25; ++m) {
        const int a = m / 5, s = m % 5, pa_ = a - 1;
        SB();
        if (a < 4) { if (s < 4) MM(7, s & 3, (a >> 1) & 1, a & 1); else acc[(a >> 1) & 1][a & 1] = mfma32(ca[(a >> 1) & 1], cb[a & 1], acc[(a >> 1) & 1][a & 1]); }
        SB();
        if (m == 0) RDN(7, 10, 4);
        if (m == 1) {
          asm volatile("s_waitcnt vmcnt(%4)" : "+v"(gm[0]), "+v"(gm[1]), "+v"(gc[0]), "+v"(gc[1]) : "n"(BEFORE(28) - GPOS - 1 + (NRL > 0 ? 1 : 0)) : "memory");
          u32 bad = ((gm[0].y ^ tag) | (gm[1].y ^ tag)) | ((gc[0].y ^ tag) | (gc[1].y ^ tag));
          PIN(bad);
          if (__builtin_expect(__builtin_amdgcn_ballot_w64(bad != 0) != 0, 0)) {
            const GAS u64* pm = (const GAS u64*)(ws + WS_ST) + 16 * (16 * mt + 8 * wt + (r32 >> 3)) + (r32 & 7);
            const GAS u64* pc = (const GAS u64*)(ws + WS_CS) + 128 * nt + 64 * wf + r32;
#pragma unroll 1
            for (int it = 0; it < SPIN_CAP; ++it) {
              const u64 m0_ = __hip_atomic_load(pm, RLX_AGENT), m1_ = __hip_atomic_load(pm + 64, RLX_AGENT);
              const u64 c0_ = __hip_atomic_load(pc, RLX_AGENT), c1_ = __hip_atomic_load(pc + 32, RLX_AGENT);
              gm[0].x = (u32)m0_; gm[1].x = (u32)m1_; gc[0].x = (u32)c0_; gc[1].x = (u32)c1_;
              if (all64((u32)(m0_ >> 32) == tag && (u32)(m1_ >> 32) == tag && (u32)(c0_ >> 32) == tag && (u32)(c1_ >> 32) == tag)) break;
            }
          }
        }
        if (m == 2) { ca[0] = v4u{h ? 0u : gc[0].x, 0u, 0u, 0u}; PIN(ca[0]); }
        if (m == 2) { cb[0] = v4u{__builtin_amdgcn_perm(gm[0].x, gm[0].x, 0x01000100u) ^ 0x80008000u, 0u, 0u, 0u}; PIN(cb[0]); }
        if (m == 3) RDN(7, 14, 4);
        if (m == 4) { cb[1] = v4u{__builtin_amdgcn_perm(gm[1].x, gm[1].x, 0x01000100u) ^ 0x80008000u, 0u, 0u, 0u}; PIN(cb[1]); }
        if (m == 5) { RDN(7, 18, 6); rec_post(crec, w, 2); }
        if (m == 10) { ca[1] = v4u{h ? 0u : gc[1].x, 0u, 0u, 0u}; PIN(ca[1]); }
        if (m == 15) cf = *(volatile LAS v4u*)(uintptr_t)crec;
        if (m < NRL) RAW(m < NRL ? RAW_BEFORE(28) + m : 0);
        if (pa_ >= 0 && s >= 1) {
          const int I = (pa_ >> 1) & 1, J = pa_ & 1, S = (s - 1) >> 1, E = 2 * ((s - 1) & 1);
          CVT(I, J, S, E); CVT(I, J, S, E + 1);
          if (E == 2) HST(I, J, S);
        }
      }
      static_assert(NRL <= 7 && NJOIN <= 56 - NFILL && NJOIN >= 28 && NRAW0 <= 32, "w2 schedule");
      SB();
      PAD(5);
      TS(10);
      if (!all64(umin(umin(cf.x, cf.y), umin(cf.z, cf.w)) >= 2u)) rec_wait4(crec, 2);
      FENCE();
      UNROLL for (int i = 0; i < NRAW0; ++i) W2_ST(i);
      SB();
      UNROLL for (int i = NRAW0; i < NJOIN; ++i) RAW(i);
      SB();
      TS(3);
      WAIT_V(NB);
      if (l == 0) __hip_atomic_store((GAS u32*)(ws + WS_FH) + FL * ((2 * mt + wt) * 32 + 2 * nt + wf), tag, RLX_AGENT);
      TS(4);
    } else {
      __builtin_amdgcn_s_setprio(0);
      u32 tch[7];
      const u32 v_t = (l >> 2) * 1024 + (l & 3) * 128;
#define TOUCH(I) tch[I] = __builtin_amdgcn_raw_buffer_load_b32(rw2, v_t, (448 * (r - 28) + 64 * (I) + 16 * w) * 1024, 0)
      TOUCH(0); RAW(0); RAW(1); RAW(2); RAW(3);
      HBAR();
      asm volatile("s_waitcnt vmcnt(5)" : "+v"(wv[0]), "+v"(wv[1]), "+v"(wv[2]), "+v"(wv[3]), "+v"(wv[4]), "+v"(wv[5]), "+v"(wv[6]), "+v"(wv[7]),
                   "+v"(wv[8]), "+v"(wv[9]), "+v"(wv[10]), "+v"(wv[11]), "+v"(wv[12]), "+v"(wv[13]), "+v"(wv[14]), "+v"(wv[15])::"memory");
      TOUCH(1); RAW(4); RAW(5); RAW(6); RAW(7);
      SB();
      float cs[8];
      u32 one_lo = 0x00003f80u, one_hi = 0x3f800000u;
      PIN(one_lo); PIN(one_hi);
      UNROLL for (int j = 0; j < 8; ++j) cs[j] = 0.f;
      UNROLL for (int i = 0; i < 16; ++i) UNROLL for (int j = 0; j < 4; ++j) { const u32 u = wv[i][j]; cs[2 * j] = dot2(u, one_lo, cs[2 * j]); cs[2 * j + 1] = dot2(u, one_hi, cs[2 * j + 1]); }
      lds_st<f32x4>(98304 + (8 * w + (l >> 3)) * 272 + 32 * (l & 7), f32x4{cs[0], cs[1], cs[2], cs[3]});
      lds_st<f32x4>(98304 + (8 * w + (l >> 3)) * 272 + 32 * (l & 7) + 16, f32x4{cs[4], cs[5], cs[6], cs[7]});
      HBAR();
      tag = __builtin_amdgcn_readfirstlane(lds_ld<u32>(M_TAG));
      {
        float s = 0.f;
        UNROLL for (int i = 0; i < 8; ++i) s += lds_ld<float>(98304 + (8 * (l >> 4) + i) * 272 + 4 * (16 * w + (l & 15)));
        const auto s16 = __builtin_amdgcn_permlane16_swap(asu(s), asu(s), false, false);
        s = asf(s16[0]) + asf(s16[1]);
        const auto s32 = __builtin_amdgcn_permlane32_swap(asu(s), asu(s), false, false);
        s = asf(s32[0]) + asf(s32[1]);
        const float hi = rbf(s);
        if (iu < 14 && l < 16) __hip_atomic_store((GAS u64*)(ws + WS_CS) + 64 * ii + 16 * w + l, ((u64)tag << 32) | pk_bf16(hi, s - hi), RLX_AGENT);
      }
      TS(2);
      UNROLL for (int i = 2; i < 7; ++i) { TOUCH(i); RAW(4 * i); RAW(4 * i + 1); RAW(4 * i + 2); RAW(4 * i + 3); NBAR(); }
      UNROLL for (int i = 0; i < NRAW0; ++i) { if (28 + i < NJOIN) RAW(28 + i < NJOIN ? 28 + i : 0); SB(); W2_ST(i); SB(); }
      UNROLL for (int i = 0; i < 7; ++i) asm volatile("" ::"v"(tch[i]));
      __builtin_amdgcn_s_waitcnt(0x0F70);
      SB();
    }
    u32 a2[2][2];
    UNROLL for (int d2 = 0; d2 < 2; ++d2) {
      a2[0][d2] = w * 8192 + (4 * h + q) * 128 + (((4 * d2 + 2 * blk + (p >> 1)) ^ (((q >> 1) & 1) << 2)) * 16) + 8 * (p & 1);
      a2[1][d2] = a2[0][d2] + 65536; PIN(a2[0][d2]); PIN(a2[1][d2]);
    }
#define W2_FRAG(K, D2) cat(lds_tr(a2[((K) >> 3) & 1][D2] + (((K) >> 2) & 1) * 32768 + ((K) & 3) * 2048), lds_tr(a2[((K) >> 3) & 1][D2] + (((K) >> 2) & 1) * 32768 + ((K) & 3) * 2048 + 1024))
    UNROLL for (int i = NJOIN; i < 56 - NFILL; ++i) { RAW(i); SB(); if (i >= NJOIN + 4 && i - NJOIN - 4 + NRAW0 < 32) { W2_ST(i - NJOIN - 4 + NRAW0); SB(); } }
    static_assert(56 - NFILL - NJOIN - 4 + NRAW0 >= 32, "w2 schedule");
    TS(5);
    const GAS u32* fhp = (const GAS u32*)(ws + WS_FH) + FL * (tt * 32 + 7 * w + (l < 7 ? l : 0));
    const u32 fh0 = __hip_atomic_load(fhp, RLX_AGENT);
    SB();
    UNROLL for (int i = 56 - NFILL; i < 56; ++i) RAW(i);
    SB();
    v4u fa[28][2];
    UNROLL for (int k = 0; k < 2; ++k) { fa[k][0] = W2_FRAG(k, 0); fa[k][1] = W2_FRAG(k, 1); }
    u32 s_h = WS_H + ((2 * tt) * 112 + 28 * w) * 1024;
    u32 v_h[2] = {(u32)(16 * l), (u32)(16 * l + 112 * 1024)};
    asm volatile("" : "+s"(s_h));
    PIN(v_h[0]); PIN(v_h[1]);
    SB();
    TS(11);
    PAD(3);
    if (!all64(fh0 == tag)) poll_flag(fhp, tag);
    AFTER_POLL();
    TS(6);
    f32x16 acc[2][2];
    UNROLL for (int i = 0; i < 2; ++i) UNROLL for (int j = 0; j < 2; ++j) UNROLL for (int e = 0; e < 16; ++e) acc[i][j][e] = 0.f;
    v4u hb[28][2];
#define H_LOAD(K, T2) hb[K][T2] = __builtin_amdgcn_raw_buffer_load_b128(rws, v_h[T2] + ((K) & 3) * 1024, s_h + ((K) >> 2) * 4096, AUX_SC1)
    UNROLL for (int k = 0; k < HWIN; ++k) { H_LOAD(k, 0); SB(); H_LOAD(k, 1); SB(); }
    SB();
    UNROLL for (int k = 0; k < 24; ++k) {
      const int kn = k + 2, st = 32 + 2 * (k - 2);
      acc[0][0] = mfma32(fa[k][0], hb[k][0], acc[0][0]);
      SB();
      fa[kn][0] = W2_FRAG(kn, 0);
      if (k >= 22) fa[kn + 2][0] = W2_FRAG(kn + 2, 0);
      SB();
      acc[1][0] = mfma32(fa[k][1], hb[k][0], acc[1][0]);
      SB();
      if (k + HWIN < 28) H_LOAD(k + HWIN, 0);
      if (k >= 2 && k < 14) W2_ST(k >= 2 && k < 14 ? st : 32);
      SB();
      acc[0][1] = mfma32(fa[k][0], hb[k][1], acc[0][1]);
      SB();
      fa[kn][1] = W2_FRAG(kn, 1);
      if (k >= 22) fa[kn + 2][1] = W2_FRAG(kn + 2, 1);
      SB();
      acc[1][1] = mfma32(fa[k][1], hb[k][1], acc[1][1]);
      SB();
      if (k + HWIN < 28) H_LOAD(k + HWIN, 1);
      if (k >= 2 && k < 14) W2_ST(k >= 2 && k < 14 ? st + 1 : 32);
      if (k == 0) TS(26);

      if (k == 14) xo = __builtin_amdgcn_raw_buffer_load_b128(RSRC(PTR(ka, 0), L * D * 2), ooff, 0, 0);
      SB();
    }
    TS(7);
    u32 pw[2][2];
    UNROLL for (int i = 0; i < 2; ++i) UNROLL for (int e = 0; e < 2; ++e) { pw[i][e] = w * 8192 + r32 * 256 + (((4 * i + 2 * e + h) ^ (r32 & 7)) * 16); PIN(pw[i][e]); }
    UNROLL for (int m = 0; m < 21; ++m) {
      const int a = m >> 2, mp = m - 5, pa_ = mp >> 2, e = mp & 3;
      SB();
      if (a < 4) acc[a & 1][(a >> 1) & 1] = mfma32(fa[24 + (m & 3)][a & 1], hb[24 + (m & 3)][(a >> 1) & 1], acc[a & 1][(a >> 1) & 1]);
      SB();
      if (mp >= 0) {
        const int i = pa_ & 1, j = (pa_ >> 1) & 1;
        const f32x4 v = {acc[i][j][4 * e], acc[i][j][4 * e + 1], acc[i][j][4 * e + 2], acc[i][j][4 * e + 3]};
        lds_st<f32x4>(pw[e >> 1][e & 1] + j * 32768 + i * 128, v);
      }
    }
    TS(8);
  }
  PAD(4);
  BAR_L();
  TS(14);
  {
    const int tok = tid >> 3, o = tid & 7;
    const u32 a = (tok >> 5) * 32768 + (tok & 31) * 256;
    const u32 c0a = a + (((2 * o) ^ (tok & 7)) * 16), c1a = a + (((2 * o + 1) ^ (tok & 7)) * 16);
    f32x4 s0 = (lds_ld<f32x4>(c0a) + lds_ld<f32x4>(c0a + 8192)) + (lds_ld<f32x4>(c0a + 16384) + lds_ld<f32x4>(c0a + 24576));
    f32x4 s1 = (lds_ld<f32x4>(c1a) + lds_ld<f32x4>(c1a + 8192)) + (lds_ld<f32x4>(c1a + 16384) + lds_ld<f32x4>(c1a + 24576));
    const float rs = lds_ld<float>(M_RS + 4 * tok);
    const v4u R = lds_ld<v4u>(H_U + tok * 144 + 16 * o);
    v4u res;
    UNROLL for (int j = 0; j < 4; ++j) {
      const u32 xj_ = xo[j], rj = R[j];
      const float bl = j < 2 ? s0[2 * (j & 1)] : s1[2 * (j & 1)], bh = j < 2 ? s0[2 * (j & 1) + 1] : s1[2 * (j & 1) + 1];
      res[j] = pk_bf16(__builtin_fmaf(bl, rs, bf_lo(xj_) + bf_lo(rj)), __builtin_fmaf(bh, rs, bf_hi(xj_) + bf_hi(rj)));
    }
    *(GAS v4u*)((GAS char*)PTR(kb, 1) + ooff) = res;
  }
  PAD(0);
}

extern "C" void kernel_launch(void* const* d_in, const int* in_sizes, int n_in, void* d_out, int out_size, void* d_ws, size_t ws_size,
                              hipStream_t stream) {
  fused_s4d_mlp<<<256, 512, 0, stream>>>((const u16*)d_in[0], (const u16*)d_in[1], (const u16*)d_in[2], (const u16*)d_in[3], (const u16*)d_in[4],
                                         (const u16*)d_in[5], (const u16*)d_in[6], (const u16*)d_in[8], (const u16*)d_in[9], (u16*)d_out,
                                         (char*)d_ws);
}
```

```cpp
#define SELMAX 0
#define XSEL 0
#include <hip/hip_runtime.h>
#include <stdint.h>

typedef unsigned short u16;
typedef unsigned u32;
typedef unsigned long long u64;
typedef u32 v4u __attribute__((ext_vector_type(4)));
typedef u32 v2u __attribute__((ext_vector_type(2)));
typedef u32 v2u_a4 __attribute__((ext_vector_type(2), aligned(4)));
typedef int v4i __attribute__((ext_vector_type(4)));
typedef int v2i __attribute__((ext_vector_type(2)));
typedef int v16i __attribute__((ext_vector_type(16)));
typedef short v4s __attribute__((ext_vector_type(4)));
typedef float f32x2 __attribute__((ext_vector_type(2)));
typedef float f32x4 __attribute__((ext_vector_type(4)));
typedef float f32x16 __attribute__((ext_vector_type(16)));
typedef __bf16 bf16x2 __attribute__((ext_vector_type(2)));
typedef __bf16 bf16x8 __attribute__((ext_vector_type(8)));
typedef _Float16 f16x2 __attribute__((ext_vector_type(2)));
typedef _Float16 f16x8 __attribute__((ext_vector_type(8)));
typedef short s16x2 __attribute__((ext_vector_type(2)));
#define LAS __attribute__((address_space(3)))
#define GAS __attribute__((address_space(1)))
#if defined(__HIP_DEVICE_COMPILE__)
#define NOPK __attribute__((target("no-packed-fp32-ops")))
#else
#define NOPK
#endif
#define DI NOPK __device__ __forceinline__
#define RLX_AGENT __ATOMIC_RELAXED, __HIP_MEMORY_SCOPE_AGENT
#define SB() __builtin_amdgcn_sched_barrier(0)
#define PIN(x) asm volatile("" : "+v"(x))
#define UNROLL _Pragma("unroll")

#define DBG 0
#define NPRE 8
#define NM1 8
#define NM2 4
#define NPRO 20
#define KGRAN 22
#define SLP 1
#define PRIO 2
#define PSLP 2
#define NRL 4
#define NB 8
#define NFILL 8
#define YB0 4
#define YB1 12
#define HPRIO 3
#define EARLY 1
#define HWIN 5
#define XSLOT 1

#define ND0 ((32 - NPRO) / 4)
#define IS_D(K) ((K) >= 3 && (K) <= 18)
#define MIN_(A, B) ((A) < (B) ? (A) : (B))
#define RAW_BEFORE(K) (2 * ((K) - MIN_(K, ND0) - ((K) < 3 ? 0 : (K) > 19 ? 16 : (K) - 3)))
#define BEFORE(K) (NPRO + 2 * (K) + 2 * MIN_(K, ND0) + ((K) > KGRAN ? 4 : 0))
#define LASTPOS(T) ((T) <= 3 ? 8 * (T) + 7 : BEFORE(4 * (T) - 10) + 1)
#define GPOS (BEFORE(KGRAN + 1) - 1)
static_assert((NPRO & 3) == 0 && NPRO >= 20 && NPRO <= 32 && KGRAN >= 18 && KGRAN < 27, "load schedule");

constexpr int L = 2048, D = 512, NS = 128, F = 1792;
constexpr int WS_NONCE = 0;
constexpr int FL = 32;
constexpr int WS_CS = 16384;
constexpr int WS_ST = 32768;
constexpr int WS_FH = 65536;
constexpr int WS_FY = WS_FH + 131072;
constexpr int WS_XN = 524288;
constexpr int WS_Y = WS_XN + L * D * 2;
constexpr int WS_H = WS_Y + L * D * 2;
constexpr int WS_END = WS_H + L * F * 2;
constexpr int WS_DBG = 16 << 20;
constexpr int HB = 131072;
constexpr int H_U = HB;
constexpr int H_S = H_U + 9216;
constexpr int H_T = H_S + 17408;
constexpr int M_CF = H_T + 2688;
constexpr int M_HF = M_CF + 1024;
constexpr int M_TAG = M_HF + 1024;
constexpr int M_RS = M_TAG + 16;
constexpr int M_ST = M_RS + 256;
constexpr int LDS_BYTES = 163840;
constexpr int SPIN_CAP = 1 << 16;
constexpr int AUX_SC1 = 16;

DI float asf(u32 v) { return __builtin_bit_cast(float, v); }
DI u32 asu(float v) { return __builtin_bit_cast(u32, v); }
DI float bf_lo(u32 v) { return asf(v << 16); }
DI float bf_hi(u32 v) { return asf(v & 0xffff0000u); }
DI u32 pk_bf16(float lo, float hi) { f32x2 v = {lo, hi}; return __builtin_bit_cast(u32, __builtin_convertvector(v, bf16x2)); }
DI float rbf(float f) { return bf_lo(pk_bf16(f, f)); }
DI u32 pk_f16(float lo, float hi) { f32x2 v = {lo, hi}; return __builtin_bit_cast(u32, __builtin_convertvector(v, f16x2)); }
DI f32x16 mfma32(v4u a, v4u b, f32x16 c) { return __builtin_amdgcn_mfma_f32_32x32x16_bf16(__builtin_bit_cast(bf16x8, a), __builtin_bit_cast(bf16x8, b), c, 0, 0, 0); }
DI f32x4 mfma16h(v4u a, v4u b, f32x4 c) { return __builtin_amdgcn_mfma_f32_16x16x32_f16(__builtin_bit_cast(f16x8, a), __builtin_bit_cast(f16x8, b), c, 0, 0, 0); }
template <class T> DI T lds_ld(u32 a) { return *(const LAS T*)(uintptr_t)a; }
template <class T> DI void lds_st(u32 a, T v) { *(LAS T*)(uintptr_t)a = v; }
DI v2u lds_tr(u32 a) { return __builtin_bit_cast(v2u, __builtin_amdgcn_ds_read_tr16_b64_v4i16((LAS v4s*)(uintptr_t)a)); }
DI v4u cat(v2u a, v2u b) { v4u r = {a.x, a.y, b.x, b.y}; return r; }
DI bool all64(bool c) { return __builtin_amdgcn_ballot_w64(c) == ~0ull; }
DI u32 umin(u32 a, u32 b) { return a < b ? a : b; }
DI u32 umax(u32 a, u32 b) { return a > b ? a : b; }
template <int CTRL, int RM> DI float dpp_add(float v) { return v + asf(__builtin_amdgcn_update_dpp(0, asu(v), CTRL, RM, 0xf, true)); }
template <int CTRL, int RM> DI u32 dpp_max(u32 v) { return umax(v, (u32)__builtin_amdgcn_update_dpp(0, (int)v, CTRL, RM, 0xf, true)); }
#define BAR_L() asm volatile("s_waitcnt lgkmcnt(0)\n\ts_barrier" ::: "memory")
#define BAR_V(N) asm volatile("s_waitcnt vmcnt(%0) lgkmcnt(0)\n\ts_barrier" ::"n"(N) : "memory")
#define HBAR() do { SB(); BAR_L(); SB(); } while (0)
#define NBAR() do { SB(); asm volatile("s_barrier" ::: "memory"); SB(); } while (0)
#define WAIT_V(N) asm volatile("s_waitcnt vmcnt(%0)" ::"n"(N) : "memory")
#define FENCE() asm volatile("" ::: "memory")
DI v4i make_srd(const void* p, u32 bytes) { v4i s; s.x = (int)(uintptr_t)p; s.y = (int)((uintptr_t)p >> 32); s.z = (int)bytes; s.w = 0x00020000; return s; }
DI void rec_post(u32 rec, int w, u32 v) { FENCE(); *(volatile LAS u32*)(uintptr_t)(rec + 4 * w) = v; FENCE(); }
DI void rec_wait4(u32 rec, u32 v) {
#pragma unroll 1
  for (int i = 0; i < SPIN_CAP * 16; ++i) {
    const v4u f = *(volatile LAS v4u*)(uintptr_t)rec;
    if (all64(umin(umin(f.x, f.y), umin(f.z, f.w)) >= v)) break;
    __builtin_amdgcn_s_sleep(SLP);
  }
  FENCE();
}
DI void rec_wait1(u32 rec, int w, u32 v) {
#pragma unroll 1
  for (int i = 0; i < SPIN_CAP * 16; ++i) {
    const u32 f = *(volatile LAS u32*)(uintptr_t)(rec + 4 * w);
    if (all64(f >= v)) break;
    __builtin_amdgcn_s_sleep(SLP);
  }
  FENCE();
}
DI void poll_flag(const GAS u32* f, u32 tag) {
#pragma unroll 1
  for (int i = 0; i < SPIN_CAP; ++i) {
    const u32 v = __hip_atomic_load(f, RLX_AGENT);
    if (all64(v == tag)) break;
    __builtin_amdgcn_s_sleep(PSLP);
  }
}
#define AFTER_POLL() do { __builtin_amdgcn_fence(__ATOMIC_ACQUIRE, "wavefront"); FENCE(); } while (0)
DI u32 poll_granule(const GAS u64* g, u32 tag) {
  u64 v = 0;
#pragma unroll 1
  for (int i = 0; i < SPIN_CAP; ++i) {
    v = __hip_atomic_load(g, RLX_AGENT);
    if (all64((u32)(v >> 32) == tag)) break;
    __builtin_amdgcn_s_sleep(1);
  }
  return (u32)v;
}
DI float dot2(u32 a, u32 b, float c) { return __builtin_amdgcn_fdot2_f32_bf16(__builtin_bit_cast(bf16x2, a), __builtin_bit_cast(bf16x2, b), c, false); }
template <int V> struct ic { static constexpr int v = V; };
template <int I, int N, class Fn> DI void static_for(Fn&& f) { if constexpr (I < N) { f(ic<I>{}); static_for<I + 1, N>(f); } }
#define SFOR(VAR, N0, N1) static_for<N0, N1>([&](auto VAR##_) NOPK __attribute__((always_inline)) { constexpr int VAR = decltype(VAR##_)::v;
#define SEND });
struct cpx { float r, i; };
DI cpx cmul(cpx a, cpx b) { cpx c; c.r = __builtin_fmaf(a.r, b.r, -(a.i * b.i)); c.i = __builtin_fmaf(a.r, b.i, a.i * b.r); return c; }
DI cpx zpow(float a2, float th, float tl, float p) {
  const float mag = __builtin_amdgcn_exp2f(a2 * p);
  const float ph = __builtin_fmaf(tl, p, __builtin_amdgcn_fractf(th * p));
  cpx z; z.r = mag * __builtin_amdgcn_cosf(ph); z.i = mag * __builtin_amdgcn_sinf(ph); return z;
}
DI float exp_f(float x) {
  const float t = x * 1.44269502162933349609375f;
  const float e = __builtin_fmaf(x, 1.925963033500011079e-08f, __builtin_fmaf(x, 1.44269502162933349609375f, -t));
  const float r = __builtin_amdgcn_exp2f(t);
  const float c = __builtin_fmaf(r * e, 0.693147180559945f, r);
  return c == c ? c : r;
}

#define TS(i) do {} while (0)

#define HTS(i) do {} while (0)
#define CTS 0
NOPK __global__ __launch_bounds__(512, 2) void fused_s4d_mlp(const u16* a_x, const u16* a_lar, const u16* a_aim, const u16* a_cre, const u16* a_cim,
                                                             const u16* a_ldt, const u16* a_dsk, const u16* a_w1, const u16* a_w2, u16* a_out, char* a_ws) {
  __shared__ __attribute__((aligned(16))) char smem[LDS_BYTES];
  asm volatile("" ::"v"((u32)(uintptr_t)(LAS char*)smem));
  const u64 t_entry = __builtin_amdgcn_s_memrealtime();
  typedef __attribute__((address_space(4))) const char* kptr;
  const kptr kas = (kptr)__builtin_amdgcn_kernarg_segment_ptr();
  const v16i ka = *(volatile __attribute__((address_space(4))) const v16i*)kas;
  const v4i kb = *(volatile __attribute__((address_space(4))) const v4i*)(kas + 64);
  const v2i kc = *(volatile __attribute__((address_space(4))) const v2i*)(kas + 80);
  SB();
#define ARGS_WAIT() SB()
#define PTR(V, I) ((char*)(((u64)(u32)(V)[2 * (I) + 1] << 32) | (u32)(V)[2 * (I)]))
#define RSRC(P, BYTES) __builtin_amdgcn_make_buffer_rsrc(P, 0, BYTES, 0x00020000)
  const int b = __builtin_amdgcn_workgroup_id_x(), xj = b & 7, r = b >> 3;
  const int tid = __builtin_amdgcn_workitem_id_x();
  const int wave = __builtin_amdgcn_readfirstlane(tid >> 6);
  const int l = tid & 63;
  const int tt = 4 * xj + (r >> 3), dt = r & 7;
  const v4u z4 = {0, 0, 0, 0};
  const u32 ooff = (64 * tt + (tid >> 3)) * 1024 + 128 * dt + 16 * (tid & 7);
  v4u xo;

  if (wave < 4) {
    const int hw = wave, rb = 32 * xj + r, c0 = 64 * xj + 2 * r, chl = hw >> 1, hm = hw & 1, ch = c0 + chl;
    const int g = l >> 4, i16 = l & 15;
    float p_lar = 0.f, p_aim = 0.f, p_cre = 0.f, p_cim = 0.f, p_cre2 = 0.f, p_cim2 = 0.f, p_ldt = 0.f, p_dsk = 0.f;
    u32 nonce;
    {
      u32 v1 = (ch * NS + 64 * hm + l) * 2, v2 = v1 ^ 128, v3 = ch * 2, v4 = 0;
      PIN(v1); PIN(v2); PIN(v3); PIN(v4);
      ARGS_WAIT();
      asm volatile("global_load_short_d16_hi %0, %9, %13\n\tglobal_load_short_d16_hi %6, %11, %17\n\tglobal_load_short_d16_hi %1, %9, %14\n\t"
                   "global_load_short_d16_hi %2, %9, %15\n\tglobal_load_short_d16_hi %3, %9, %16\n\tglobal_load_short_d16_hi %4, %10, %15\n\t"
                   "global_load_short_d16_hi %5, %10, %16\n\tglobal_load_short_d16_hi %7, %11, %18\n\tglobal_load_dword %8, %12, %19 sc1"
                   : "+&v"(p_lar), "+&v"(p_aim), "+&v"(p_cre), "+&v"(p_cim), "+&v"(p_cre2), "+&v"(p_cim2), "+&v"(p_ldt), "+&v"(p_dsk), "=&v"(nonce)
                   : "v"(v1), "v"(v2), "v"(v3), "v"(v4), "s"(PTR(ka, 1)), "s"(PTR(ka, 2)), "s"(PTR(ka, 3)), "s"(PTR(ka, 4)), "s"(PTR(ka, 5)),
                     "s"(PTR(ka, 6)), "s"(PTR(kc, 0)) : "memory");
    }
    char* const ws = PTR(kc, 0);
    const __amdgpu_buffer_rsrc_t rws = RSRC(ws, WS_END);
    TS(12);
    if (hw >= 2) lds_st<v4u>(M_CF + 16 * (tid - 128), z4);
    const u32 hrec = M_HF + 16 * l;
    const u32 tb = H_T + chl * 1344 + 16;
    u32 xa[17];
    {
      const int tau = 64 * hm + l, jtop = 2047 - 16 * tau;
      float fj = (float)jtop;
      PIN(fj);
      const bool ident = tau >= 112;
      const u32 sel = ident ? 0u : 0x1fffeu, base = ident ? tb + 2 * jtop : tb - 2 * 0x4280, stp = ident ? 2u : 0u;
      UNROLL for (int e = 0; e < 18; e += 2) {
        const u32 bits = pk_bf16(fj - (float)e, fj - (float)(e + 1));
        xa[e] = ((bits << 1) & sel) + (base - e * stp);
        if (e < 16) xa[e + 1] = ((bits >> 15) & sel) + (base - (e + 1) * stp);
      }
      UNROLL for (int e = 0; e < 17; ++e) PIN(xa[e]);
    }
    u32 sw[4];
    UNROLL for (int i = 0; i < 4; ++i) { sw[i] = H_S + hw * 4352 + 4 * l + 1088 * i; PIN(sw[i]); }
    u32 sr = H_S + hw * 4352 + i16 * 272 + 16 * g;
    u32 lnr = H_U + (i16 >> 2) * 1088 + (128 * hw + 16 * g + 4 * (l & 3)) * 2;
    u32 xno = WS_XN + rb * 8192 + (128 * hw + l) * 16;
    u32 uoff = WS_XN + (64 * hw + (l >> 1)) * 8192 + (c0 + (l & 1)) * 16;
    u32 ta0 = tb + 2 * (128 * (g >> 1) + 4 * (g & 1) + 8 * i16), ta1 = tb + 2 * (g < 2 ? 256 + 64 * g + 4 * i16 : 384 + 64 * (g - 2) + 2 * i16);
    u32 ta1b = ta1 + (g < 2 ? 4 : 64), ta2 = g < 3 ? tb + 2 * (512 + 64 * g + i16) : tb - 2;
    PIN(sr); PIN(lnr); PIN(xno); PIN(uoff); PIN(ta0); PIN(ta1); PIN(ta1b); PIN(ta2);
    TS(10);
    asm volatile("s_waitcnt vmcnt(0)" : "+v"(p_lar), "+v"(p_aim), "+v"(p_cre), "+v"(p_cim), "+v"(p_cre2), "+v"(p_cim2), "+v"(p_ldt), "+v"(p_dsk), "+v"(nonce)::"memory");
    TS(11);
    const u32 tag = __builtin_amdgcn_readfirstlane(nonce) * 0x9E3779B1u + 0x7F4A7C15u;
    if (tid == 0) lds_st<u32>(M_TAG, tag);
    if (tid == 0) __hip_atomic_store((GAS u64*)(ws + 400000) + b, ((u64)tag << 32) | (u32)t_entry, RLX_AGENT);
    const float dtv = rbf(exp_f(p_ldt));
    const float alpha = -rbf(exp_f(p_lar)) * dtv, beta = p_aim * dtv;
    const float a2 = alpha * 1.44269504088896341f;
    const float th = beta * 0.15915494309189535f;
    const float tl = __builtin_fmaf(beta, 6.42063831672e-09f, __builtin_fmaf(beta, 0.15915494309189535f, -th));
    u32 mx = umax(umax(asu(p_cre) & 0x7fffffffu, asu(p_cim) & 0x7fffffffu), umax(asu(p_cre2) & 0x7fffffffu, asu(p_cim2) & 0x7fffffffu));
    mx = dpp_max<0xB1, 0xf>(mx); mx = dpp_max<0x4E, 0xf>(mx); mx = dpp_max<0x124, 0xf>(mx); mx = dpp_max<0x128, 0xf>(mx);
    mx = dpp_max<0x142, 0xa>(mx); mx = dpp_max<0x143, 0xc>(mx);
    int ex = (int)(__builtin_amdgcn_readlane(mx, 63) >> 23) + 1;
    ex = ex < 32 ? 32 : (ex > 230 ? 230 : ex);
    const float cscale = asf((u32)(254 + 14 - ex) << 23);
    const float yscale = asf((u32)(ex - 17) << 23) * dtv;
    cpx z8c = zpow(a2, th, tl, 8.f);
    z8c.i = -z8c.i;
    u32 tgb[4];
    UNROLL for (int j = 0; j < 4; ++j) tgb[j] = ((tag >> (6 * j)) & 7u) | (((tag >> (6 * j + 3)) & 7u) << 16);
    u32 an[57];
#define KEEP(I, Z) do { const cpx z_ = (Z); an[I] = pk_f16(z_.r, z_.i); PIN(an[I]); } while (0)
    cpx e0[16], e1[16];
    cpx y; y.r = p_cre * cscale; y.i = -p_cim * cscale;
    UNROLL for (int i = 0; i < YB0; ++i) { KEEP(i, y); y = cmul(y, z8c); }
    HTS(16);
    HBAR();
    {
      const u32 stv = lds_ld<u32>(M_ST + 4 * (l & 7));
      v4u o[2];
      UNROLL for (int k = 0; k < 2; ++k) o[k] = cat(lds_tr(lnr + 128 * k), lds_tr(lnr + 128 * k + 4 * 1088));
      SB();
      UNROLL for (int i = YB0; i < YB0 + 4; ++i) { KEEP(i, y); y = cmul(y, z8c); }
      SB();
      UNROLL for (int k = 0; k < 2; ++k) {
        UNROLL for (int j = 0; j < 4; ++j) o[k][j] = (o[k][j] & ~0x00070007u) | tgb[j];
        __builtin_amdgcn_raw_buffer_store_b128(o[k], rws, xno + 1024 * k, 0, AUX_SC1);
      }
      if (tid < 8) __hip_atomic_store((GAS u64*)(ws + WS_ST) + 16 * rb + tid, ((u64)tag << 32) | stv, RLX_AGENT);
      HTS(1);
      UNROLL for (int i = YB0 + 4; i < YB1; ++i) { KEEP(i, y); if (i < 15) y = cmul(y, z8c); }
    }
    HTS(17);
    NBAR();
    UNROLL for (int i = YB1; i < 16; ++i) { KEEP(i, y); if (i < 15) y = cmul(y, z8c); }
    const cpx z1 = zpow(a2, th, tl, 1.f);
    e0[0].r = 32768.f; e0[0].i = 0.f;
    e0[1].r = z1.r * 32768.f; e0[1].i = z1.i * 32768.f;
    UNROLL for (int i = 2; i < 8; ++i) e0[i] = cmul(e0[i - 1], z1);
    UNROLL for (int i = 0; i < 8; ++i) KEEP(16 + i, e0[i]);
    HTS(18);
    NBAR();
    const cpx z128 = zpow(a2, th, tl, 128.f);
    e0[8].r = z128.r * 32768.f; e0[8].i = z128.i * 32768.f;
    UNROLL for (int i = 9; i < 16; ++i) e0[i] = cmul(e0[i - 8], z128);
    UNROLL for (int i = 8; i < 16; ++i) KEEP(16 + i, e0[i]);
    const cpx z256 = cmul(z128, z128), z512 = cmul(z256, z256);
    UNROLL for (int i = 0; i < 2; ++i) { e1[i] = cmul(e0[8 * (i >> 2) + 2 * (i & 3)], z256); KEEP(32 + i, e1[i]); }
    HTS(19);
    NBAR();
    UNROLL for (int i = 2; i < 8; ++i) { e1[i] = cmul(e0[8 * (i >> 2) + 2 * (i & 3)], z256); KEEP(32 + i, e1[i]); }
    UNROLL for (int i = 0; i < 4; ++i) { e1[8 + i] = cmul(e0[8 * (i >> 1) + 4 * (i & 1)], z512); KEEP(40 + i, e1[8 + i]); }
    UNROLL for (int i = 0; i < 4; ++i) { e1[12 + i] = cmul(e1[4 * (i >> 1) + 2 * (i & 1)], z512); KEEP(44 + i, e1[12 + i]); }
    HTS(20);
    NBAR();
    {
      const cpx z1024 = zpow(a2, th, tl, 1024.f);
      cpx e2;
      e2.r = z1024.r * 32768.f; e2.i = z1024.i * 32768.f;
      KEEP(48, e2); KEEP(49, cmul(e0[8], z1024)); KEEP(50, cmul(e1[0], z1024)); KEEP(51, cmul(e1[4], z1024));
      KEEP(52, cmul(e1[8], z1024)); KEEP(53, cmul(e1[10], z1024)); KEEP(54, cmul(e1[12], z1024)); KEEP(55, cmul(e1[14], z1024));
      KEEP(56, cmul(e2, z1024));
    }
    v4u uv[2];
    UNROLL for (int i = 0; i < 2; ++i) uv[i] = __builtin_amdgcn_raw_buffer_load_b128(rws, uoff + i * 32 * 8192, 0, AUX_SC1);
    HTS(21);
    NBAR();
    __builtin_amdgcn_s_setprio(HPRIO);
    const u32 kx = H_S + (hw | 1) * 4352 + 16 * l;
    f32x4 kacc[3];
    {
      v4u yb[4], xf[3][4];
      if (EARLY == 0) NBAR();
      UNROLL for (int i = 0; i < 16; ++i) lds_st<u32>(sw[i >> 2] + 272 * (i & 3), an[i]);
      UNROLL for (int s = 0; s < 4; ++s) yb[s] = lds_ld<v4u>(sr + 64 * s);
      UNROLL for (int t = 0; t < 3; ++t) {
        if (EARLY == t + 1) NBAR();
        UNROLL for (int i = 0; i < (t < 2 ? 16 : 9); ++i) lds_st<u32>(sw[i >> 2] + 272 * (i & 3), an[16 + 16 * t + i]);
        UNROLL for (int s = 0; s < 4; ++s) xf[t][s] = lds_ld<v4u>(sr + 64 * s);
        if (t > 0) {
          SB();
          kacc[t - 1] = f32x4{0.f, 0.f, 0.f, 0.f};
          UNROLL for (int s = 0; s < 4; ++s) kacc[t - 1] = mfma16h(xf[t - 1][s], yb[s], kacc[t - 1]);
          SB();
        }
      }
      kacc[2] = f32x4{0.f, 0.f, 0.f, 0.f};
      UNROLL for (int s = 0; s < 4; ++s) kacc[2] = mfma16h(xf[2][s], yb[s], kacc[2]);
    }
    TS(24);
#define U_PUT() do { UNROLL for (int i = 0; i < 2; ++i) { \
      _Pragma("unroll 1") for (int it = 0; it < SPIN_CAP; ++it) { \
        UNROLL for (int j = 0; j < 4; ++j) uv[i][j] ^= tgb[j]; \
        if ((((uv[i].x | uv[i].y) | (uv[i].z | uv[i].w)) & 0x00070007u) == 0) break; \
        __builtin_amdgcn_s_sleep(PSLP); \
        FENCE(); \
        uv[i] = __builtin_amdgcn_raw_buffer_load_b128(rws, uoff + i * 32 * 8192, 0, AUX_SC1); \
      } \
      lds_st<v4u>(uw + 512 * i, uv[i]); } } while (0)
    const u32 uw = H_U + (l & 1) * 4608 + 512 + (64 * hw + (l >> 1)) * 16;
    if (hm) {
      UNROLL for (int i = 0; i < 3; ++i) lds_st<f32x4>(kx + 1024 * i, kacc[i]);
      rec_post(hrec, hw, 2);
      if (l < 32) lds_st<v4u>(H_U + chl * 4608 + 16 * l, z4);
      rec_wait1(hrec, hw ^ 1, 2);
    } else {
      rec_wait1(hrec, hw ^ 1, 2);
      u32 kp[3][2];
      UNROLL for (int i = 0; i < 3; ++i) {
        const f32x4 v = (kacc[i] + lds_ld<f32x4>(kx + 1024 * i)) * 4.76837158203125e-07f;
        kp[i][0] = pk_f16(v[0], v[1]); kp[i][1] = pk_f16(v[2], v[3]);
      }
      lds_st<v2u>(ta0, v2u{kp[0][0], kp[0][1]});
      lds_st<u32>(ta1, kp[1][0]); lds_st<u32>(ta1b, kp[1][1]);
      lds_st<u16>(ta2, (u16)(g < 3 ? kp[2][0] : 0u));
      if (g < 2) { lds_st<u16>(ta2 + 32, (u16)(kp[2][0] >> 16)); lds_st<u16>(ta2 + 64, (u16)kp[2][1]); lds_st<u16>(ta2 + 96, (u16)(kp[2][1] >> 16)); }
      rec_post(hrec, hw, 2);
    }
    TS(3);
    const u32 eb = H_S + chl * 8704;
    {
      u32 kv[17];
      UNROLL for (int e = 0; e < 17; ++e) kv[e] = lds_ld<u16>(xa[e]);
      SB();
      U_PUT();
      SB();
      v4u ea0, ea1, eb0, eb1;
      UNROLL for (int e = 0; e < 4; ++e) {
        ea0[e] = kv[2 * e] | (kv[2 * e + 1] << 16); ea1[e] = kv[2 * e + 8] | (kv[2 * e + 9] << 16);
        eb0[e] = kv[2 * e + 1] | (kv[2 * e + 2] << 16); eb1[e] = kv[2 * e + 9] | (kv[2 * e + 10] << 16);
      }
      const u32 a = eb + 32 * (64 * hm + l);
      lds_st<v4u>(a, ea0); lds_st<v4u>(a + 16, ea1); lds_st<v4u>(a + 4160, eb0); lds_st<v4u>(a + 4176, eb1);
      if (l < 4 && hm == 0) lds_st<v4u>(eb + 4096 + (l >> 1) * 4160 + 16 * (l & 1), z4);
    }
    rec_post(hrec, hw, 4);
    u32 fa4[4];
    {
      const int x0 = 2047 - 16 * (1 + 8 * hm) - i16 + 8 * g;
      const u32 a = eb + ((i16 & 1) ? 0 : 4160) + 4 * (x0 >> 1) - 512 * 7 - 192;
      UNROLL for (int i = 0; i < 4; ++i) { fa4[i] = a + 1024 * (3 - i); PIN(fa4[i]); }
    }
#define FA_LD(W, MI) cat(*(const LAS v2u_a4*)(uintptr_t)(fa4[(W) >> 1] + 512 * (1 - ((W) & 1)) + 192 - 64 * (MI)), \
                         *(const LAS v2u_a4*)(uintptr_t)(fa4[(W) >> 1] + 512 * (1 - ((W) & 1)) + 192 - 64 * (MI) + 8))
    v4u FA[4][8], G[4][8];
    TS(4);
    rec_wait4(hrec, 4);
    if (b == 0 && tid == 0) __hip_atomic_store((GAS u32*)(ws + WS_NONCE), tag, RLX_AGENT);
    TS(5);
    f32x4 acc[8];
    UNROLL for (int i = 0; i < 8; ++i) acc[i] = f32x4{0.f, 0.f, 0.f, 0.f};
    const u32 ub = H_U + chl * 4608 + 512;
    v2u uu[4];
    u32 pf;
    {
      const u32 a = ub + 32 * i16 + 16 * g - 32 * (1 + 8 * hm) - 192;
      G[0][0] = lds_ld<v4u>(a + 192);
      UNROLL for (int w = 0; w < 8; ++w) FA[0][w] = FA_LD(w, 0);
      UNROLL for (int v = 1; v < 8; ++v) G[0][v] = lds_ld<v4u>(a + 512 * v + 192);
      UNROLL for (int mi = 0; mi < 4; ++mi) {
        UNROLL for (int v = 0; v < 8; ++v) {
          SB();
          UNROLL for (int w = 0; v + w < 8; ++w) {
            acc[v + w] = mfma16h(FA[mi][w], G[mi][v], acc[v + w]);
            if (w == 0 && mi < 3) { SB(); G[mi + 1][v] = lds_ld<v4u>(a + 512 * v + 192 - 64 * (mi + 1)); SB(); }
            if (mi < 3 && v < 4 && (w == 1 || w == 3)) { SB(); FA[mi + 1][2 * v + (w >> 1)] = FA_LD(2 * v + (w >> 1), mi + 1); SB(); }
            if (mi == 3 && w == 0 && v < 4) { SB(); uu[v] = lds_ld<v2u>(ub + 2 * (256 * (4 * hm + v) + 16 * i16 + 4 * g)); SB(); }
            if (mi == 3 && w == 0 && v == 4) { SB(); pf = *(volatile LAS u32*)(uintptr_t)(hrec + 4 * (hw ^ 1)); SB(); }
          }
          if (mi == 2 && v == 4) rec_post(hrec, hw, 5);
        }
      }
    }
    TS(6);
    {
      const u32 xw = H_S + hw * 4352 + 16 * l, xrd = H_S + (hw ^ 1) * 4352 + 16 * l;
      if (!all64(pf >= 5)) rec_wait1(hrec, hw ^ 1, 5);
      FENCE();
      UNROLL for (int i = 0; i < 4; ++i) { const f32x4 lo = acc[i], hi = acc[4 + i]; lds_st<f32x4>(xw + 1024 * i, hm ? lo : hi); acc[i] = hm ? hi : lo; }
      rec_post(hrec, hw, 6);
      const float dsv = p_dsk * (1.0f / 1024.0f);
      u32 yo = WS_Y + ch * 4096 + hm * 2048 + 16 * l;
      PIN(yo);
      UNROLL for (int i = 0; i < 4; ++i) {
        const u32 ux = uu[i].x, uy = uu[i].y;
        const f16x2 u01 = __builtin_bit_cast(f16x2, ux), u23 = __builtin_bit_cast(f16x2, uy);
        acc[i][0] = __builtin_fmaf((float)u01.x, dsv, acc[i][0] * yscale); acc[i][1] = __builtin_fmaf((float)u01.y, dsv, acc[i][1] * yscale);
        acc[i][2] = __builtin_fmaf((float)u23.x, dsv, acc[i][2] * yscale); acc[i][3] = __builtin_fmaf((float)u23.y, dsv, acc[i][3] * yscale);
        PIN(acc[i]);
      }
      rec_wait1(hrec, hw ^ 1, 6);
      u32 o[4][2];
      UNROLL for (int i = 0; i < 4; ++i) {
        const f32x4 y = lds_ld<f32x4>(xrd + 1024 * i);
        o[i][0] = pk_bf16(__builtin_fmaf(y[0], yscale, acc[i][0]), __builtin_fmaf(y[1], yscale, acc[i][1]));
        o[i][1] = pk_bf16(__builtin_fmaf(y[2], yscale, acc[i][2]), __builtin_fmaf(y[3], yscale, acc[i][3]));
      }
      UNROLL for (int i = 0; i < 4; i += 2) {
        const auto s0 = __builtin_amdgcn_permlane16_swap(o[i][0], o[i + 1][0], false, false);
        const auto s1 = __builtin_amdgcn_permlane16_swap(o[i][1], o[i + 1][1], false, false);
        const v4u v = {s0[0], s1[0], s0[1], s1[1]};
        __builtin_amdgcn_raw_buffer_store_b128(v, rws, yo + (i >> 1) * 1024, 0, AUX_SC1);
      }
      rec_post(hrec, hw, 7);
      TS(2);
      WAIT_V(0);
      if (l == 0) __hip_atomic_store((GAS u32*)(ws + WS_FY) + FL * (4 * (c0 >> 1) + hw), tag, RLX_AGENT);
    }
    TS(7);
    xo = __builtin_amdgcn_raw_buffer_load_b128(RSRC(PTR(ka, 0), L * D * 2), ooff, 0, 0);
    if (hw == 0) lds_st<float>(M_RS + 4 * l, __builtin_amdgcn_rcpf(bf_hi(poll_granule((const GAS u64*)(ws + WS_ST) + 16 * (8 * tt + (l >> 3)) + (l & 7), tag))));
    poll_flag((const GAS u32*)(ws + WS_FY) + FL * (4 * (32 * dt + 8 * hw + (i16 >> 1)) + 2 * (l & 1) + (tt >> 4)), tag);
    AFTER_POLL();
    TS(8);
    {
      v4u yv[2];
      const int pc = l & 7;
      UNROLL for (int i = 0; i < 2; ++i)
        yv[i] = __builtin_amdgcn_raw_buffer_load_b128(rws, WS_Y + (64 * dt + 16 * hw + 8 * i + (l >> 3)) * 4096 + (tt >> 3) * 1024 +
                                                     (2 * (pc >> 2) + ((tt >> 2) & 1)) * 256 + (4 * (tt & 3) + (pc & 3)) * 16, 0, AUX_SC1);
      rec_wait4(hrec, 7);
      const u32 yst = H_S + hw * 4352;
      UNROLL for (int i = 0; i < 2; ++i) lds_st<v4u>(yst + (8 * i + (l >> 3)) * 192 + 32 * (pc & 3) + 16 * (pc >> 2), yv[i]);
      const u32 a = yst + (i16 >> 2) * 192 + 32 * g + 8 * (l & 3);
      const v4u y0 = cat(lds_tr(a), lds_tr(a + 4 * 192)), y1 = cat(lds_tr(a + 8 * 192), lds_tr(a + 12 * 192));
      lds_st<v4u>(H_U + l * 144 + 32 * hw, y0);
      lds_st<v4u>(H_U + l * 144 + 32 * hw + 16, y1);
    }
    TS(9);
  } else {
    __builtin_amdgcn_s_setprio(PRIO);
    const int w = wave - 4, wf = w & 1, wt = w >> 1;
    const int h = l >> 5, r32 = l & 31, blk = (l >> 4) & 1, q = (l & 15) >> 2, p = l & 3;
    const int mt = 4 * (xj >> 1) + (r & 3), nt = 7 * (xj & 1) + (r >> 2);
    u32 v_x[4], v_w1[4], v_l = 16 * l;
    UNROLL for (int i = 0; i < 4; ++i) {
      v_x[i] = (l >> 3) * 1024 + (((l & 7) ^ (l >> 4) ^ (4 * (i & 1))) * 16) + i * 7168;
      v_w1[i] = (l >> 4) * 3584 + (((l & 15) ^ ((l >> 4) << 2)) * 16) + i * (4 * 3584 - 1024);
      PIN(v_x[i]); PIN(v_w1[i]);
    }
    u32 s_x = (128 * mt + 32 * w) * 1024, s_w1 = (16 * w) * 3584 + 256 * nt, s_r = (256 * xj + 8 * r + 2 * w) * 1024;
    const int iu = 4 * (xj >> 1) + r - 28, ii = 14 * (xj & 1) + (iu < 14 ? iu : 0);
    asm volatile("" : "+s"(s_x), "+s"(s_w1), "+s"(s_r));
    PIN(v_l);
    ARGS_WAIT();
    TS(25);
    char* const ws = PTR(kc, 0);
    const v4i sx = {ka[0], ka[1], L * D * 2, 0x00020000}, sw1 = {ka[14], ka[15], D * F * 2, 0x00020000}, sws = {kc[0], kc[1], WS_END, 0x00020000};
    v4u xr[2];
    asm volatile("buffer_load_dwordx4 %0, %2, %3, %4 offen\n\tbuffer_load_dwordx4 %1, %2, %3, %4 offen offset:1024"
                 : "=&v"(xr[0]), "=&v"(xr[1]) : "v"(v_l), "s"(sx), "s"(s_r) : "memory");
    u32 so;
#define SET_M0(V) asm volatile("s_mov_b32 m0, %1" : "+s"(so) : "s"(V) : "memory")
#define GLDS(VO, SRD, I) asm volatile("buffer_load_dwordx4 %0, %1, %2 offen offset:%3 lds" ::"v"(VO), "s"(SRD), "s"(so), "n"((I) * 1024) : "memory")
#define SETG(N) do { const int G_ = (N) >> 2, T_ = G_ >> 1; \
        if (G_ & 1) { so = s_w1 + T_ * (64 * 3584); SET_M0((T_ & 3) * 32768 + 16384 + w * 4096); } \
        else { so = s_x + T_ * 128; SET_M0((T_ & 3) * 32768 + w * 4096); } } while (0)
#define GL(N) do { if (((N) >> 2) & 1) GLDS(v_w1[(N) & 3], sw1, (N) & 3); else GLDS(v_x[(N) & 3], sx, (N) & 3); } while (0)
#define GLRUN(N0, N1) do { UNROLL for (int n_ = (N0); n_ < (N1); ++n_) { if ((n_ & 3) == 0) { SETG(n_); asm volatile("s_nop 0"); } GL(n_); } } while (0)
    v4u wv[16];
    const u32 v_c = (l >> 3) * 3584 + (l & 7) * 16;
#define CSRUN(N0, N1) do { UNROLL for (int i_ = (N0); i_ < (N1); ++i_) { const u32 sof_ = (128 * w + 8 * i_) * 3584 + 128 * ii; \
        asm volatile("buffer_load_dwordx4 %0, %1, %2, %3 offen" : "=&v"(wv[i_]) : "v"(v_c), "s"(sw1), "s"(sof_) : "memory"); } } while (0)
    if (r < 28) GLRUN(0, NPRE); else CSRUN(0, NPRE);
    const __amdgpu_buffer_rsrc_t rws = RSRC(ws, WS_END), rw2 = RSRC(PTR(kb, 0), F * D * 2);
    const u32 v_w2 = (l >> 3) * 1024 + (l & 7) * 16;
    v4u wr[56];
#define RAW(I) wr[I] = __builtin_amdgcn_raw_buffer_load_b128(rw2, v_w2, (448 * w + 8 * (I)) * 1024 + 128 * dt, 0)
    u32 ab[2][4], aa[2][2];
    {
      const int row = 64 * wt + r32, z = (row >> 1) & 7;
      UNROLL for (int s = 0; s < 4; ++s) { ab[0][s] = row * 128 + (((2 * s + h) ^ z) * 16); ab[1][s] = ab[0][s] + 65536; PIN(ab[0][s]); PIN(ab[1][s]); }
      UNROLL for (int f2 = 0; f2 < 2; ++f2) {
        aa[0][f2] = 16384 + (8 * h + q) * 256 + (((8 * wf + 4 * f2 + 2 * blk + (p >> 1)) ^ (q << 2)) * 16) + 8 * (p & 1);
        aa[1][f2] = aa[0][f2] + 65536; PIN(aa[0][f2]); PIN(aa[1][f2]);
      }
    }
    u32 wa[2], lnw = H_U + 2 * w * 1088 + 16 * l;
    wa[0] = w * 8192 + (l >> 3) * 128 + (((l & 7) ^ (((l >> 4) & 1) << 2)) * 16); wa[1] = wa[0] + 65536;
    PIN(wa[0]); PIN(wa[1]); PIN(lnw);
#define W2_ST(I) lds_st<v4u>(wa[((I) >> 4) & 1] + (((I) >> 3) & 1) * 32768 + ((I) & 7) * 1024, wr[I])
    TS(9);
    asm volatile("s_waitcnt vmcnt(%2)" : "+v"(xr[0]), "+v"(xr[1]) : "n"(NPRE) : "memory");
    TS(12);
    {
      float s[2], qq[2], d[2][8], m[2], var[2];
      UNROLL for (int k = 0; k < 2; ++k) {
        s[k] = 0.f; qq[k] = 0.f;
        UNROLL for (int j = 0; j < 4; ++j) { const u32 u = xr[k][j]; s[k] = dot2(u, 0x3f803f80u, s[k]); qq[k] = dot2(u, u, qq[k]); }
      }
#define RED4(C, RM) do { s[0] = dpp_add<C, RM>(s[0]); qq[0] = dpp_add<C, RM>(qq[0]); s[1] = dpp_add<C, RM>(s[1]); qq[1] = dpp_add<C, RM>(qq[1]); } while (0)
      RED4(0xB1, 0xf); RED4(0x4E, 0xf); RED4(0x124, 0xf); RED4(0x128, 0xf); RED4(0x142, 0xa); RED4(0x143, 0xc);
      SB();
      if (r < 28) GLRUN(NPRE, NPRE + NM1); else CSRUN(NPRE, 16);
      SB();
      float e1[2];
      bool redo = false;
      UNROLL for (int k = 0; k < 2; ++k) {
        e1[k] = asf(__builtin_amdgcn_readlane(asu(s[k]), 63)) * (1.0f / D);
        const float e2 = asf(__builtin_amdgcn_readlane(asu(qq[k]), 63)) * (1.0f / D);
        var[k] = __builtin_fmaf(-e1[k], e1[k], e2);
        redo |= !(var[k] * 16.f >= e2);
        m[k] = rbf(e1[k]);
        UNROLL for (int j = 0; j < 4; ++j) { const u32 u = xr[k][j]; d[k][2 * j] = bf_lo(u) - m[k]; d[k][2 * j + 1] = bf_hi(u) - m[k]; }
      }
      if (__builtin_amdgcn_readfirstlane(redo)) {
        UNROLL for (int k = 0; k < 2; ++k) {
          s[k] = 0.f; qq[k] = 0.f;
          UNROLL for (int j = 0; j < 8; ++j) { s[k] += d[k][j]; qq[k] = __builtin_fmaf(d[k][j], d[k][j], qq[k]); }
        }
        RED4(0xB1, 0xf); RED4(0x4E, 0xf); RED4(0x124, 0xf); RED4(0x128, 0xf); RED4(0x142, 0xa); RED4(0x143, 0xc);
        UNROLL for (int k = 0; k < 2; ++k) {
          const float c1 = asf(__builtin_amdgcn_readlane(asu(s[k]), 63)) * (1.0f / D), c2 = asf(__builtin_amdgcn_readlane(asu(qq[k]), 63)) * (1.0f / D);
          var[k] = __builtin_fmaxf(__builtin_fmaf(-c1, c1, c2), 0.f);
        }
      }
      u32 pay[2];
      UNROLL for (int k = 0; k < 2; ++k) {
        const float sd = rbf(__builtin_amdgcn_sqrtf(rbf(rbf(var[k]) + 1e-5f)));
        const float rs = 1024.0f * __builtin_amdgcn_rcpf(sd);
        v4u o;
        UNROLL for (int j = 0; j < 4; ++j) {
          const u32 dd = pk_bf16(d[k][2 * j], d[k][2 * j + 1]);
          const u32 n = pk_bf16(bf_lo(dd) * rs, bf_hi(dd) * rs);
          o[j] = pk_f16(bf_lo(n), bf_hi(n));
        }
        lds_st<v4u>(lnw + k * 1088, o);
        pay[k] = (asu(m[k]) >> 16) | (asu(sd) & 0xffff0000u);
        if (k == 0) { SB(); if (r < 28) GLRUN(NPRE + NM1, NPRE + NM1 + NM2); SB(); }
      }
      lds_st<v2u>(M_ST + 8 * w, v2u{pay[0], pay[1]});
    }
    SB();
    TS(13);
    u32 tag;
    constexpr int NRAW0 = RAW_BEFORE(28) + NRL;
    constexpr int NJOIN = NRAW0 + NB;
    if (r < 28) {
      GLRUN(NPRE + NM1 + NM2, NPRO);
      const u32 crec = M_CF + 16 * l;
      f32x16 acc[2][2];
      UNROLL for (int i = 0; i < 2; ++i) UNROLL for (int j = 0; j < 2; ++j) UNROLL for (int e = 0; e < 16; ++e) acc[i][j][e] = 0.f;
      v4u fb[2][4][2];
      v2u fl[2][4][2], fh[2][4][2];
      v2u gm[2], gc[2];
#define RD(T, s_, j_) do { const int hi_ = ((T) >> 1) & 1, o_ = ((T) & 1) * 32768, u_ = (T) & 1; \
        if ((j_) == 0) fb[u_][s_][0] = lds_ld<v4u>(ab[hi_][s_] + o_); \
        if ((j_) == 1) fl[u_][s_][0] = lds_tr(aa[hi_][0] + o_ + (s_) * 4096); \
        if ((j_) == 2) fh[u_][s_][0] = lds_tr(aa[hi_][0] + o_ + (s_) * 4096 + 1024); \
        if ((j_) == 3) fb[u_][s_][1] = lds_ld<v4u>(ab[hi_][s_] + o_ + 4096); \
        if ((j_) == 4) fl[u_][s_][1] = lds_tr(aa[hi_][1] + o_ + (s_) * 4096); \
        if ((j_) == 5) fh[u_][s_][1] = lds_tr(aa[hi_][1] + o_ + (s_) * 4096 + 1024); } while (0)
#define RDN(T, J0, N) do { UNROLL for (int j2_ = (J0); j2_ < (J0) + (N); ++j2_) { \
        if ((T) < 7) RD(T, j2_ / 6, j2_ % 6); \
        else if (j2_ < 12) RD(T, j2_ / 3, j2_ % 3); else if (j2_ < 16) RD(T, (j2_ - 12) & 3, 3); else RD(T, ((j2_ - 16) >> 1) & 3, 4 + (j2_ & 1)); } } while (0)
#define MM(T, S, I, J) acc[I][J] = mfma32(cat(fl[(T) & 1][S][I], fh[(T) & 1][S][I]), fb[(T) & 1][S][J], acc[I][J])
#define SLOT(K, J) do { if ((K) < ND0) { GL(NPRO + 4 * (K) + 2 * (J)); GL(NPRO + 4 * (K) + 2 * (J) + 1); } \
        else if (IS_D(K)) GL(26 + 2 * (K) + (J)); else RAW(RAW_BEFORE(K) + (J)); } while (0)
#define NEXTG(K) do { if ((K) < ND0) SETG(NPRO + 4 * (K)); else if (IS_D(K) && ((K) & 1)) SETG(26 + 2 * (K)); } while (0)
      NEXTG(0);
      u32 v_g = r32 * 8, v_m = (r32 >> 3) * 128 + (r32 & 7) * 8, ho[2];
      UNROLL for (int j = 0; j < 2; ++j) { ho[j] = WS_H + ((4 * mt + 2 * wt + j) * 112 + 8 * nt + 4 * wf) * 1024 + 16 * l; PIN(ho[j]); }
      PIN(v_g); PIN(v_m);
      TS(16);
      BAR_V(NPRO - 8);
      TS(1);
      RDN(0, 0, 10);
      v4u ca[2], cb[2], cf;
      u32 tagv;
      SFOR(k, 0, 28)
        constexpr int t = k >> 2, s = k & 3;
        SB();
        MM(t, s, 0, 0);
        SB();
        if (s == 3) {
          if (t < 5) BAR_V(BEFORE(k) - LASTPOS(t + 1) - 1);
          if (t == 5) BAR_V(BEFORE(k) - LASTPOS(7) - 1);
          if (CTS || t == 3 || t == 5 || t == 6) TS(17 + t);
          RDN(t + 1, 0, 2);
        }
        SLOT(k, 0);
        SB();
        MM(t, s, 0, 1);
        SB();
        if (s == 3) RDN(t + 1, 2, 4);
        if (s == 0) RDN(t, 10, 4);
        if (s == 1) RDN(t, 17, 4);
        if (k == 2) tagv = lds_ld<u32>(M_TAG);
        SB();
        MM(t, s, 1, 0);
        SB();
        SLOT(k, 1);
        SB();
        MM(t, s, 1, 1);
        SB();
        if (s == 3) RDN(t + 1, 6, 4);
        if (s == 0) RDN(t, 14, 3);
        if (s == 1) RDN(t, 21, 3);
        if (k == KGRAN) {
          const u32 so_m = WS_ST + (16 * mt + 8 * wt) * 128, so_c = WS_CS + (128 * nt + 64 * wf) * 8;
          asm volatile("buffer_load_dwordx2 %0, %8, %5, %6 offen sc1\n\tbuffer_load_dwordx2 %1, %8, %5, %6 offen offset:512 sc1\n\t"
                       "buffer_load_dwordx2 %2, %4, %5, %7 offen sc1\n\tbuffer_load_dwordx2 %3, %4, %5, %7 offen offset:256 sc1"
                       : "=&v"(gm[0]), "=&v"(gm[1]), "=&v"(gc[0]), "=&v"(gc[1]) : "v"(v_g), "s"(sws), "s"(so_m), "s"(so_c), "v"(v_m) : "memory");
        }
        if (k == 6) tag = __builtin_amdgcn_readfirstlane(tagv);
        NEXTG(k + 1);
      SEND
      v4u ho_;
#define CVT(I, J, S, E) do { const s16x2 v_ = __builtin_bit_cast(s16x2, pk_bf16(acc[I][J][8 * (S) + 2 * (E)], acc[I][J][8 * (S) + 2 * (E) + 1])); \
        const s16x2 z_ = {0, 0}; ho_[E] = __builtin_bit_cast(u32, __builtin_elementwise_max(v_, z_)); } while (0)
#define HST(I, J, S) __builtin_amdgcn_raw_buffer_store_b128(ho_, rws, ho[J] + (2 * (I) + (S)) * 1024, 0, AUX_SC1)
      UNROLL for (int m = 0; m < 25; ++m) {
        const int a = m / 5, s = m % 5, pa_ = a - 1;
        SB();
        if (a < 4) { if (s < 4) MM(7, s & 3, (a >> 1) & 1, a & 1); else acc[(a >> 1) & 1][a & 1] = mfma32(ca[(a >> 1) & 1], cb[a & 1], acc[(a >> 1) & 1][a & 1]); }
        SB();
        if (m == 0) RDN(7, 10, 4);
        if (m == 1) {
          asm volatile("s_waitcnt vmcnt(%4)" : "+v"(gm[0]), "+v"(gm[1]), "+v"(gc[0]), "+v"(gc[1]) : "n"(BEFORE(28) - GPOS - 1 + (NRL > 0 ? 1 : 0)) : "memory");
          u32 bad = ((gm[0].y ^ tag) | (gm[1].y ^ tag)) | ((gc[0].y ^ tag) | (gc[1].y ^ tag));
          PIN(bad);
          if (__builtin_expect(__builtin_amdgcn_ballot_w64(bad != 0) != 0, 0)) {
            const GAS u64* pm = (const GAS u64*)(ws + WS_ST) + 16 * (16 * mt + 8 * wt + (r32 >> 3)) + (r32 & 7);
            const GAS u64* pc = (const GAS u64*)(ws + WS_CS) + 128 * nt + 64 * wf + r32;
#pragma unroll 1
            for (int it = 0; it < SPIN_CAP; ++it) {
              const u64 m0_ = __hip_atomic_load(pm, RLX_AGENT), m1_ = __hip_atomic_load(pm + 64, RLX_AGENT);
              const u64 c0_ = __hip_atomic_load(pc, RLX_AGENT), c1_ = __hip_atomic_load(pc + 32, RLX_AGENT);
              gm[0].x = (u32)m0_; gm[1].x = (u32)m1_; gc[0].x = (u32)c0_; gc[1].x = (u32)c1_;
              if (all64((u32)(m0_ >> 32) == tag && (u32)(m1_ >> 32) == tag && (u32)(c0_ >> 32) == tag && (u32)(c1_ >> 32) == tag)) break;
            }
          }
        }
        if (m == 2) { ca[0] = v4u{h ? 0u : gc[0].x, 0u, 0u, 0u}; PIN(ca[0]); }
        if (m == 2) { cb[0] = v4u{__builtin_amdgcn_perm(gm[0].x, gm[0].x, 0x01000100u) ^ 0x80008000u, 0u, 0u, 0u}; PIN(cb[0]); }
        if (m == 3) RDN(7, 14, 4);
        if (m == 4) { cb[1] = v4u{__builtin_amdgcn_perm(gm[1].x, gm[1].x, 0x01000100u) ^ 0x80008000u, 0u, 0u, 0u}; PIN(cb[1]); }
        if (m == 5) { RDN(7, 18, 6); rec_post(crec, w, 2); }
        if (m == 10) { ca[1] = v4u{h ? 0u : gc[1].x, 0u, 0u, 0u}; PIN(ca[1]); }
        if (m == 15) cf = *(volatile LAS v4u*)(uintptr_t)crec;
        if (m < NRL) RAW(m < NRL ? RAW_BEFORE(28) + m : 0);
        if (pa_ >= 0 && s >= 1) {
          const int I = (pa_ >> 1) & 1, J = pa_ & 1, S = (s - 1) >> 1, E = 2 * ((s - 1) & 1);
          CVT(I, J, S, E); CVT(I, J, S, E + 1);
          if (E == 2) HST(I, J, S);
        }
      }
      static_assert(NRL <= 7 && NJOIN <= 56 - NFILL && NJOIN >= 28 && NRAW0 <= 32, "w2 schedule");
      SB();
      TS(10);
      if (!all64(umin(umin(cf.x, cf.y), umin(cf.z, cf.w)) >= 2u)) rec_wait4(crec, 2);
      FENCE();
      UNROLL for (int i = 0; i < NRAW0; ++i) W2_ST(i);
      SB();
      UNROLL for (int i = NRAW0; i < NJOIN; ++i) RAW(i);
      SB();
      TS(3);
      WAIT_V(NB);
      if (l == 0) __hip_atomic_store((GAS u32*)(ws + WS_FH) + FL * ((2 * mt + wt) * 32 + 2 * nt + wf), tag, RLX_AGENT);
      TS(4);
    } else {
      __builtin_amdgcn_s_setprio(0);
      u32 tch[7];
      const u32 v_t = (l >> 2) * 1024 + (l & 3) * 128;
#define TOUCH(I) tch[I] = __builtin_amdgcn_raw_buffer_load_b32(rw2, v_t, (448 * (r - 28) + 64 * (I) + 16 * w) * 1024, 0)
      TOUCH(0); RAW(0); RAW(1); RAW(2); RAW(3);
      HBAR();
      asm volatile("s_waitcnt vmcnt(5)" : "+v"(wv[0]), "+v"(wv[1]), "+v"(wv[2]), "+v"(wv[3]), "+v"(wv[4]), "+v"(wv[5]), "+v"(wv[6]), "+v"(wv[7]),
                   "+v"(wv[8]), "+v"(wv[9]), "+v"(wv[10]), "+v"(wv[11]), "+v"(wv[12]), "+v"(wv[13]), "+v"(wv[14]), "+v"(wv[15])::"memory");
      TOUCH(1); RAW(4); RAW(5); RAW(6); RAW(7);
      SB();
      float cs[8];
      u32 one_lo = 0x00003f80u, one_hi = 0x3f800000u;
      PIN(one_lo); PIN(one_hi);
      UNROLL for (int j = 0; j < 8; ++j) cs[j] = 0.f;
      UNROLL for (int i = 0; i < 16; ++i) UNROLL for (int j = 0; j < 4; ++j) { const u32 u = wv[i][j]; cs[2 * j] = dot2(u, one_lo, cs[2 * j]); cs[2 * j + 1] = dot2(u, one_hi, cs[2 * j + 1]); }
      lds_st<f32x4>(98304 + (8 * w + (l >> 3)) * 272 + 32 * (l & 7), f32x4{cs[0], cs[1], cs[2], cs[3]});
      lds_st<f32x4>(98304 + (8 * w + (l >> 3)) * 272 + 32 * (l & 7) + 16, f32x4{cs[4], cs[5], cs[6], cs[7]});
      HBAR();
      tag = __builtin_amdgcn_readfirstlane(lds_ld<u32>(M_TAG));
      {
        float s = 0.f;
        UNROLL for (int i = 0; i < 8; ++i) s += lds_ld<float>(98304 + (8 * (l >> 4) + i) * 272 + 4 * (16 * w + (l & 15)));
        const auto s16 = __builtin_amdgcn_permlane16_swap(asu(s), asu(s), false, false);
        s = asf(s16[0]) + asf(s16[1]);
        const auto s32 = __builtin_amdgcn_permlane32_swap(asu(s), asu(s), false, false);
        s = asf(s32[0]) + asf(s32[1]);
        const float hi = rbf(s);
        if (iu < 14 && l < 16) __hip_atomic_store((GAS u64*)(ws + WS_CS) + 64 * ii + 16 * w + l, ((u64)tag << 32) | pk_bf16(hi, s - hi), RLX_AGENT);
      }
      TS(2);
      UNROLL for (int i = 2; i < 7; ++i) { TOUCH(i); RAW(4 * i); RAW(4 * i + 1); RAW(4 * i + 2); RAW(4 * i + 3); NBAR(); }
      UNROLL for (int i = 0; i < NRAW0; ++i) { if (28 + i < NJOIN) RAW(28 + i < NJOIN ? 28 + i : 0); SB(); W2_ST(i); SB(); }
      UNROLL for (int i = 0; i < 7; ++i) asm volatile("" ::"v"(tch[i]));
      __builtin_amdgcn_s_waitcnt(0x0F70);
      SB();
    }
    u32 a2[2][2];
    UNROLL for (int d2 = 0; d2 < 2; ++d2) {
      a2[0][d2] = w * 8192 + (4 * h + q) * 128 + (((4 * d2 + 2 * blk + (p >> 1)) ^ (((q >> 1) & 1) << 2)) * 16) + 8 * (p & 1);
      a2[1][d2] = a2[0][d2] + 65536; PIN(a2[0][d2]); PIN(a2[1][d2]);
    }
#define W2_FRAG(K, D2) cat(lds_tr(a2[((K) >> 3) & 1][D2] + (((K) >> 2) & 1) * 32768 + ((K) & 3) * 2048), lds_tr(a2[((K) >> 3) & 1][D2] + (((K) >> 2) & 1) * 32768 + ((K) & 3) * 2048 + 1024))
    UNROLL for (int i = NJOIN; i < 56 - NFILL; ++i) { RAW(i); SB(); if (i >= NJOIN + 4 && i - NJOIN - 4 + NRAW0 < 32) { W2_ST(i - NJOIN - 4 + NRAW0); SB(); } }
    static_assert(56 - NFILL - NJOIN - 4 + NRAW0 >= 32, "w2 schedule");
    TS(5);
    const GAS u32* fhp = (const GAS u32*)(ws + WS_FH) + FL * (tt * 32 + 7 * w + (l < 7 ? l : 0));
    const u32 fh0 = __hip_atomic_load(fhp, RLX_AGENT);
    SB();
    UNROLL for (int i = 56 - NFILL; i < 56; ++i) RAW(i);
    SB();
    v4u fa[28][2];
    UNROLL for (int k = 0; k < 2; ++k) { fa[k][0] = W2_FRAG(k, 0); fa[k][1] = W2_FRAG(k, 1); }
    u32 s_h = WS_H + ((2 * tt) * 112 + 28 * w) * 1024;
    u32 v_h[2] = {(u32)(16 * l), (u32)(16 * l + 112 * 1024)};
    asm volatile("" : "+s"(s_h));
    PIN(v_h[0]); PIN(v_h[1]);
    SB();
    TS(11);
    if (!all64(fh0 == tag)) poll_flag(fhp, tag);
    AFTER_POLL();
    TS(6);
    f32x16 acc[2][2];
    UNROLL for (int i = 0; i < 2; ++i) UNROLL for (int j = 0; j < 2; ++j) UNROLL for (int e = 0; e < 16; ++e) acc[i][j][e] = 0.f;
    v4u hb[28][2];
#define H_LOAD(K, T2) hb[K][T2] = __builtin_amdgcn_raw_buffer_load_b128(rws, v_h[T2] + ((K) & 3) * 1024, s_h + ((K) >> 2) * 4096, AUX_SC1)
    UNROLL for (int k = 0; k < HWIN; ++k) { H_LOAD(k, 0); SB(); H_LOAD(k, 1); SB(); }
    SB();
    UNROLL for (int k = 0; k < 24; ++k) {
      const int kn = k + 2, st = 32 + 2 * (k - 2);
      acc[0][0] = mfma32(fa[k][0], hb[k][0], acc[0][0]);
      SB();
      fa[kn][0] = W2_FRAG(kn, 0);
      if (k >= 22) fa[kn + 2][0] = W2_FRAG(kn + 2, 0);
      SB();
      acc[1][0] = mfma32(fa[k][1], hb[k][0], acc[1][0]);
      SB();
      if (k + HWIN < 28) H_LOAD(k + HWIN, 0);
      if (k >= 2 && k < 14) W2_ST(k >= 2 && k < 14 ? st : 32);
      SB();
      acc[0][1] = mfma32(fa[k][0], hb[k][1], acc[0][1]);
      SB();
      fa[kn][1] = W2_FRAG(kn, 1);
      if (k >= 22) fa[kn + 2][1] = W2_FRAG(kn + 2, 1);
      SB();
      acc[1][1] = mfma32(fa[k][1], hb[k][1], acc[1][1]);
      SB();
      if (k + HWIN < 28) H_LOAD(k + HWIN, 1);
      if (k >= 2 && k < 14) W2_ST(k >= 2 && k < 14 ? st + 1 : 32);
      if (k == 0) TS(26);

      if (k == 14) xo = __builtin_amdgcn_raw_buffer_load_b128(RSRC(PTR(ka, 0), L * D * 2), ooff, 0, 0);
      SB();
    }
    TS(7);
    u32 pw[2][2];
    UNROLL for (int i = 0; i < 2; ++i) UNROLL for (int e = 0; e < 2; ++e) { pw[i][e] = w * 8192 + r32 * 256 + (((4 * i + 2 * e + h) ^ (r32 & 7)) * 16); PIN(pw[i][e]); }
    UNROLL for (int m = 0; m < 21; ++m) {
      const int a = m >> 2, mp = m - 5, pa_ = mp >> 2, e = mp & 3;
      SB();
      if (a < 4) acc[a & 1][(a >> 1) & 1] = mfma32(fa[24 + (m & 3)][a & 1], hb[24 + (m & 3)][(a >> 1) & 1], acc[a & 1][(a >> 1) & 1]);
      SB();
      if (mp >= 0) {
        const int i = pa_ & 1, j = (pa_ >> 1) & 1;
        const f32x4 v = {acc[i][j][4 * e], acc[i][j][4 * e + 1], acc[i][j][4 * e + 2], acc[i][j][4 * e + 3]};
        lds_st<f32x4>(pw[e >> 1][e & 1] + j * 32768 + i * 128, v);
      }
    }
    TS(8);
  }
  BAR_L();
  TS(14);
  {
    const int tok = tid >> 3, o = tid & 7;
    const u32 a = (tok >> 5) * 32768 + (tok & 31) * 256;
    const u32 c0a = a + (((2 * o) ^ (tok & 7)) * 16), c1a = a + (((2 * o + 1) ^ (tok & 7)) * 16);
    f32x4 s0 = (lds_ld<f32x4>(c0a) + lds_ld<f32x4>(c0a + 8192)) + (lds_ld<f32x4>(c0a + 16384) + lds_ld<f32x4>(c0a + 24576));
    f32x4 s1 = (lds_ld<f32x4>(c1a) + lds_ld<f32x4>(c1a + 8192)) + (lds_ld<f32x4>(c1a + 16384) + lds_ld<f32x4>(c1a + 24576));
    const float rs = lds_ld<float>(M_RS + 4 * tok);
    const v4u R = lds_ld<v4u>(H_U + tok * 144 + 16 * o);
    v4u res;
    UNROLL for (int j = 0; j < 4; ++j) {
      const u32 xj_ = xo[j], rj = R[j];
      const float bl = j < 2 ? s0[2 * (j & 1)] : s1[2 * (j & 1)], bh = j < 2 ? s0[2 * (j & 1) + 1] : s1[2 * (j & 1) + 1];
      res[j] = pk_bf16(__builtin_fmaf(bl, rs, bf_lo(xj_) + bf_lo(rj)), __builtin_fmaf(bh, rs, bf_hi(xj_) + bf_hi(rj)));
    }
    *(GAS v4u*)((GAS char*)PTR(kb, 1) + ooff) = res;
  }
  {
    const u32 tg = __builtin_amdgcn_readfirstlane(lds_ld<u32>(M_TAG));
    int best = SELMAX ? -(1 << 30) : (1 << 30);
    UNROLL for (int i = 0; i < 4; ++i) {
      const int dlt = (int)(poll_granule((const GAS u64*)(PTR(kc, 0) + 400000) + 64 * i + l, tg) - (u32)t_entry);
      if (XSEL < 0 || (l & 7) == XSEL) best = SELMAX ? (dlt > best ? dlt : best) : (dlt < best ? dlt : best);
    }
    for (int i = 0; i < 64; ++i) { const int o = __builtin_amdgcn_readlane(best, i); best = SELMAX ? (o > best ? o : best) : (o < best ? o : best); }
    const int bb = __builtin_amdgcn_readfirstlane(best);
    for (int i_ = 0; i_ < 100000; ++i_) { if ((int)(u32)(__builtin_amdgcn_s_memrealtime() - t_entry) - bb >= 4000) break; __builtin_amdgcn_s_sleep(1); }
  }
}

extern "C" void kernel_launch(void* const* d_in, const int* in_sizes, int n_in, void* d_out, int out_size, void* d_ws, size_t ws_size,
                              hipStream_t stream) {
  fused_s4d_mlp<<<256, 512, 0, stream>>>((const u16*)d_in[0], (const u16*)d_in[1], (const u16*)d_in[2], (const u16*)d_in[3], (const u16*)d_in[4],
                                         (const u16*)d_in[5], (const u16*)d_in[6], (const u16*)d_in[8], (const u16*)d_in[9], (u16*)d_out,
                                         (char*)d_ws);
}
```

```cpp
#define PADAT 0
#define PADT 4000
#include <hip/hip_runtime.h>
#include <stdint.h>

typedef unsigned short u16;
typedef unsigned u32;
typedef unsigned long long u64;
typedef u32 v4u __attribute__((ext_vector_type(4)));
typedef u32 v2u __attribute__((ext_vector_type(2)));
typedef u32 v2u_a4 __attribute__((ext_vector_type(2), aligned(4)));
typedef int v4i __attribute__((ext_vector_type(4)));
typedef int v2i __attribute__((ext_vector_type(2)));
typedef int v16i __attribute__((ext_vector_type(16)));
typedef short v4s __attribute__((ext_vector_type(4)));
typedef float f32x2 __attribute__((ext_vector_type(2)));
typedef float f32x4 __attribute__((ext_vector_type(4)));
typedef float f32x16 __attribute__((ext_vector_type(16)));
typedef __bf16 bf16x2 __attribute__((ext_vector_type(2)));
typedef __bf16 bf16x8 __attribute__((ext_vector_type(8)));
typedef _Float16 f16x2 __attribute__((ext_vector_type(2)));
typedef _Float16 f16x8 __attribute__((ext_vector_type(8)));
typedef short s16x2 __attribute__((ext_vector_type(2)));
#define LAS __attribute__((address_space(3)))
#define GAS __attribute__((address_space(1)))
#if defined(__HIP_DEVICE_COMPILE__)
#define NOPK __attribute__((target("no-packed-fp32-ops")))
#else
#define NOPK
#endif
#define DI NOPK __device__ __forceinline__
#define RLX_AGENT __ATOMIC_RELAXED, __HIP_MEMORY_SCOPE_AGENT
#define SB() __builtin_amdgcn_sched_barrier(0)
#define PIN(x) asm volatile("" : "+v"(x))
#define UNROLL _Pragma("unroll")

#define DBG 0
#define NPRE 8
#define NM1 8
#define NM2 4
#define NPRO 20
#define KGRAN 22
#define SLP 1
#define PRIO 2
#define PSLP 2
#define NRL 4
#define NB 8
#define NFILL 8
#define YB0 4
#define YB1 12
#define HPRIO 3
#define EARLY 1
#define HWIN 5
#define XSLOT 1

#define ND0 ((32 - NPRO) / 4)
#define IS_D(K) ((K) >= 3 && (K) <= 18)
#define MIN_(A, B) ((A) < (B) ? (A) : (B))
#define RAW_BEFORE(K) (2 * ((K) - MIN_(K, ND0) - ((K) < 3 ? 0 : (K) > 19 ? 16 : (K) - 3)))
#define BEFORE(K) (NPRO + 2 * (K) + 2 * MIN_(K, ND0) + ((K) > KGRAN ? 4 : 0))
#define LASTPOS(T) ((T) <= 3 ? 8 * (T) + 7 : BEFORE(4 * (T) - 10) + 1)
#define GPOS (BEFORE(KGRAN + 1) - 1)
static_assert((NPRO & 3) == 0 && NPRO >= 20 && NPRO <= 32 && KGRAN >= 18 && KGRAN < 27, "load schedule");

constexpr int L = 2048, D = 512, NS = 128, F = 1792;
constexpr int WS_NONCE = 0;
constexpr int FL = 32;
constexpr int WS_CS = 16384;
constexpr int WS_ST = 32768;
constexpr int WS_FH = 65536;
constexpr int WS_FY = WS_FH + 131072;
constexpr int WS_XN = 524288;
constexpr int WS_Y = WS_XN + L * D * 2;
constexpr int WS_H = WS_Y + L * D * 2;
constexpr int WS_END = WS_H + L * F * 2;
constexpr int WS_DBG = 16 << 20;
constexpr int HB = 131072;
constexpr int H_U = HB;
constexpr int H_S = H_U + 9216;
constexpr int H_T = H_S + 17408;
constexpr int M_CF = H_T + 2688;
constexpr int M_HF = M_CF + 1024;
constexpr int M_TAG = M_HF + 1024;
constexpr int M_RS = M_TAG + 16;
constexpr int M_ST = M_RS + 256;
constexpr int LDS_BYTES = 163840;
constexpr int SPIN_CAP = 1 << 16;
constexpr int AUX_SC1 = 16;

DI float asf(u32 v) { return __builtin_bit_cast(float, v); }
DI u32 asu(float v) { return __builtin_bit_cast(u32, v); }
DI float bf_lo(u32 v) { return asf(v << 16); }
DI float bf_hi(u32 v) { return asf(v & 0xffff0000u); }
DI u32 pk_bf16(float lo, float hi) { f32x2 v = {lo, hi}; return __builtin_bit_cast(u32, __builtin_convertvector(v, bf16x2)); }
DI float rbf(float f) { return bf_lo(pk_bf16(f, f)); }
DI u32 pk_f16(float lo, float hi) { f32x2 v = {lo, hi}; return __builtin_bit_cast(u32, __builtin_convertvector(v, f16x2)); }
DI f32x16 mfma32(v4u a, v4u b, f32x16 c) { return __builtin_amdgcn_mfma_f32_32x32x16_bf16(__builtin_bit_cast(bf16x8, a), __builtin_bit_cast(bf16x8, b), c, 0, 0, 0); }
DI f32x4 mfma16h(v4u a, v4u b, f32x4 c) { return __builtin_amdgcn_mfma_f32_16x16x32_f16(__builtin_bit_cast(f16x8, a), __builtin_bit_cast(f16x8, b), c, 0, 0, 0); }
template <class T> DI T lds_ld(u32 a) { return *(const LAS T*)(uintptr_t)a; }
template <class T> DI void lds_st(u32 a, T v) { *(LAS T*)(uintptr_t)a = v; }
DI v2u lds_tr(u32 a) { return __builtin_bit_cast(v2u, __builtin_amdgcn_ds_read_tr16_b64_v4i16((LAS v4s*)(uintptr_t)a)); }
DI v4u cat(v2u a, v2u b) { v4u r = {a.x, a.y, b.x, b.y}; return r; }
DI bool all64(bool c) { return __builtin_amdgcn_ballot_w64(c) == ~0ull; }
DI u32 umin(u32 a, u32 b) { return a < b ? a : b; }
DI u32 umax(u32 a, u32 b) { return a > b ? a : b; }
template <int CTRL, int RM> DI float dpp_add(float v) { return v + asf(__builtin_amdgcn_update_dpp(0, asu(v), CTRL, RM, 0xf, true)); }
template <int CTRL, int RM> DI u32 dpp_max(u32 v) { return umax(v, (u32)__builtin_amdgcn_update_dpp(0, (int)v, CTRL, RM, 0xf, true)); }
#define BAR_L() asm volatile("s_waitcnt lgkmcnt(0)\n\ts_barrier" ::: "memory")
#define BAR_V(N) asm volatile("s_waitcnt vmcnt(%0) lgkmcnt(0)\n\ts_barrier" ::"n"(N) : "memory")
#define HBAR() do { SB(); BAR_L(); SB(); } while (0)
#define NBAR() do { SB(); asm volatile("s_barrier" ::: "memory"); SB(); } while (0)
#define WAIT_V(N) asm volatile("s_waitcnt vmcnt(%0)" ::"n"(N) : "memory")
#define FENCE() asm volatile("" ::: "memory")
DI v4i make_srd(const void* p, u32 bytes) { v4i s; s.x = (int)(uintptr_t)p; s.y = (int)((uintptr_t)p >> 32); s.z = (int)bytes; s.w = 0x00020000; return s; }
DI void rec_post(u32 rec, int w, u32 v) { FENCE(); *(volatile LAS u32*)(uintptr_t)(rec + 4 * w) = v; FENCE(); }
DI void rec_wait4(u32 rec, u32 v) {
#pragma unroll 1
  for (int i = 0; i < SPIN_CAP * 16; ++i) {
    const v4u f = *(volatile LAS v4u*)(uintptr_t)rec;
    if (all64(umin(umin(f.x, f.y), umin(f.z, f.w)) >= v)) break;
    __builtin_amdgcn_s_sleep(SLP);
  }
  FENCE();
}
DI void rec_wait1(u32 rec, int w, u32 v) {
#pragma unroll 1
  for (int i = 0; i < SPIN_CAP * 16; ++i) {
    const u32 f = *(volatile LAS u32*)(uintptr_t)(rec + 4 * w);
    if (all64(f >= v)) break;
    __builtin_amdgcn_s_sleep(SLP);
  }
  FENCE();
}
DI void poll_flag(const GAS u32* f, u32 tag) {
#pragma unroll 1
  for (int i = 0; i < SPIN_CAP; ++i) {
    const u32 v = __hip_atomic_load(f, RLX_AGENT);
    if (all64(v == tag)) break;
    __builtin_amdgcn_s_sleep(PSLP);
  }
}
#define AFTER_POLL() do { __builtin_amdgcn_fence(__ATOMIC_ACQUIRE, "wavefront"); FENCE(); } while (0)
DI u32 poll_granule(const GAS u64* g, u32 tag) {
  u64 v = 0;
#pragma unroll 1
  for (int i = 0; i < SPIN_CAP; ++i) {
    v = __hip_atomic_load(g, RLX_AGENT);
    if (all64((u32)(v >> 32) == tag)) break;
    __builtin_amdgcn_s_sleep(1);
  }
  return (u32)v;
}
DI float dot2(u32 a, u32 b, float c) { return __builtin_amdgcn_fdot2_f32_bf16(__builtin_bit_cast(bf16x2, a), __builtin_bit_cast(bf16x2, b), c, false); }
template <int V> struct ic { static constexpr int v = V; };
template <int I, int N, class Fn> DI void static_for(Fn&& f) { if constexpr (I < N) { f(ic<I>{}); static_for<I + 1, N>(f); } }
#define SFOR(VAR, N0, N1) static_for<N0, N1>([&](auto VAR##_) NOPK __attribute__((always_inline)) { constexpr int VAR = decltype(VAR##_)::v;
#define SEND });
struct cpx { float r, i; };
DI cpx cmul(cpx a, cpx b) { cpx c; c.r = __builtin_fmaf(a.r, b.r, -(a.i * b.i)); c.i = __builtin_fmaf(a.r, b.i, a.i * b.r); return c; }
DI cpx zpow(float a2, float th, float tl, float p) {
  const float mag = __builtin_amdgcn_exp2f(a2 * p);
  const float ph = __builtin_fmaf(tl, p, __builtin_amdgcn_fractf(th * p));
  cpx z; z.r = mag * __builtin_amdgcn_cosf(ph); z.i = mag * __builtin_amdgcn_sinf(ph); return z;
}
DI float exp_f(float x) {
  const float t = x * 1.44269502162933349609375f;
  const float e = __builtin_fmaf(x, 1.925963033500011079e-08f, __builtin_fmaf(x, 1.44269502162933349609375f, -t));
  const float r = __builtin_amdgcn_exp2f(t);
  const float c = __builtin_fmaf(r * e, 0.693147180559945f, r);
  return c == c ? c : r;
}

#define TS(i) do {} while (0)

#define HTS(i) do {} while (0)
#define CTS 0
NOPK __global__ __launch_bounds__(512, 2) void fused_s4d_mlp(const u16* a_x, const u16* a_lar, const u16* a_aim, const u16* a_cre, const u16* a_cim,
                                                             const u16* a_ldt, const u16* a_dsk, const u16* a_w1, const u16* a_w2, u16* a_out, char* a_ws) {
  __shared__ __attribute__((aligned(16))) char smem[LDS_BYTES];
  asm volatile("" ::"v"((u32)(uintptr_t)(LAS char*)smem));
  const u64 t_entry = __builtin_amdgcn_s_memrealtime();
#define PAD(ID) do { if (PADAT == (ID)) { for (int i_ = 0; i_ < 100000; ++i_) { if (__builtin_amdgcn_s_memrealtime() - t_entry >= PADT) break; __builtin_amdgcn_s_sleep(1); } } } while (0)
  PAD(1);
  typedef __attribute__((address_space(4))) const char* kptr;
  const kptr kas = (kptr)__builtin_amdgcn_kernarg_segment_ptr();
  const v16i ka = *(volatile __attribute__((address_space(4))) const v16i*)kas;
  const v4i kb = *(volatile __attribute__((address_space(4))) const v4i*)(kas + 64);
  const v2i kc = *(volatile __attribute__((address_space(4))) const v2i*)(kas + 80);
  SB();
#define ARGS_WAIT() SB()
#define PTR(V, I) ((char*)(((u64)(u32)(V)[2 * (I) + 1] << 32) | (u32)(V)[2 * (I)]))
#define RSRC(P, BYTES) __builtin_amdgcn_make_buffer_rsrc(P, 0, BYTES, 0x00020000)
  const int b = __builtin_amdgcn_workgroup_id_x(), xj = b & 7, r = b >> 3;
  const int tid = __builtin_amdgcn_workitem_id_x();
  const int wave = __builtin_amdgcn_readfirstlane(tid >> 6);
  const int l = tid & 63;
  const int tt = 4 * xj + (r >> 3), dt = r & 7;
  const v4u z4 = {0, 0, 0, 0};
  const u32 ooff = (64 * tt + (tid >> 3)) * 1024 + 128 * dt + 16 * (tid & 7);
  v4u xo;

  if (wave < 4) {
    const int hw = wave, rb = 32 * xj + r, c0 = 64 * xj + 2 * r, chl = hw >> 1, hm = hw & 1, ch = c0 + chl;
    const int g = l >> 4, i16 = l & 15;
    float p_lar = 0.f, p_aim = 0.f, p_cre = 0.f, p_cim = 0.f, p_cre2 = 0.f, p_cim2 = 0.f, p_ldt = 0.f, p_dsk = 0.f;
    u32 nonce;
    {
      u32 v1 = (ch * NS + 64 * hm + l) * 2, v2 = v1 ^ 128, v3 = ch * 2, v4 = 0;
      PIN(v1); PIN(v2); PIN(v3); PIN(v4);
      ARGS_WAIT();
      asm volatile("global_load_short_d16_hi %0, %9, %13\n\tglobal_load_short_d16_hi %6, %11, %17\n\tglobal_load_short_d16_hi %1, %9, %14\n\t"
                   "global_load_short_d16_hi %2, %9, %15\n\tglobal_load_short_d16_hi %3, %9, %16\n\tglobal_load_short_d16_hi %4, %10, %15\n\t"
                   "global_load_short_d16_hi %5, %10, %16\n\tglobal_load_short_d16_hi %7, %11, %18\n\tglobal_load_dword %8, %12, %19 sc1"
                   : "+&v"(p_lar), "+&v"(p_aim), "+&v"(p_cre), "+&v"(p_cim), "+&v"(p_cre2), "+&v"(p_cim2), "+&v"(p_ldt), "+&v"(p_dsk), "=&v"(nonce)
                   : "v"(v1), "v"(v2), "v"(v3), "v"(v4), "s"(PTR(ka, 1)), "s"(PTR(ka, 2)), "s"(PTR(ka, 3)), "s"(PTR(ka, 4)), "s"(PTR(ka, 5)),
                     "s"(PTR(ka, 6)), "s"(PTR(kc, 0)) : "memory");
    }
    char* const ws = PTR(kc, 0);
    const __amdgpu_buffer_rsrc_t rws = RSRC(ws, WS_END);
    TS(12);
    if (hw >= 2) lds_st<v4u>(M_CF + 16 * (tid - 128), z4);
    const u32 hrec = M_HF + 16 * l;
    const u32 tb = H_T + chl * 1344 + 16;
    u32 xa[17];
    {
      const int tau = 64 * hm + l, jtop = 2047 - 16 * tau;
      float fj = (float)jtop;
      PIN(fj);
      const bool ident = tau >= 112;
      const u32 sel = ident ? 0u : 0x1fffeu, base = ident ? tb + 2 * jtop : tb - 2 * 0x4280, stp = ident ? 2u : 0u;
      UNROLL for (int e = 0; e < 18; e += 2) {
        const u32 bits = pk_bf16(fj - (float)e, fj - (float)(e + 1));
        xa[e] = ((bits << 1) & sel) + (base - e * stp);
        if (e < 16) xa[e + 1] = ((bits >> 15) & sel) + (base - (e + 1) * stp);
      }
      UNROLL for (int e = 0; e < 17; ++e) PIN(xa[e]);
    }
    u32 sw[4];
    UNROLL for (int i = 0; i < 4; ++i) { sw[i] = H_S + hw * 4352 + 4 * l + 1088 * i; PIN(sw[i]); }
    u32 sr = H_S + hw * 4352 + i16 * 272 + 16 * g;
    u32 lnr = H_U + (i16 >> 2) * 1088 + (128 * hw + 16 * g + 4 * (l & 3)) * 2;
    u32 xno = WS_XN + rb * 8192 + (128 * hw + l) * 16;
    u32 uoff = WS_XN + (64 * hw + (l >> 1)) * 8192 + (c0 + (l & 1)) * 16;
    u32 ta0 = tb + 2 * (128 * (g >> 1) + 4 * (g & 1) + 8 * i16), ta1 = tb + 2 * (g < 2 ? 256 + 64 * g + 4 * i16 : 384 + 64 * (g - 2) + 2 * i16);
    u32 ta1b = ta1 + (g < 2 ? 4 : 64), ta2 = g < 3 ? tb + 2 * (512 + 64 * g + i16) : tb - 2;
    PIN(sr); PIN(lnr); PIN(xno); PIN(uoff); PIN(ta0); PIN(ta1); PIN(ta1b); PIN(ta2);
    TS(10);
    asm volatile("s_waitcnt vmcnt(0)" : "+v"(p_lar), "+v"(p_aim), "+v"(p_cre), "+v"(p_cim), "+v"(p_cre2), "+v"(p_cim2), "+v"(p_ldt), "+v"(p_dsk), "+v"(nonce)::"memory");
    TS(11);
    const u32 tag = __builtin_amdgcn_readfirstlane(nonce) * 0x9E3779B1u + 0x7F4A7C15u;
    if (tid == 0) lds_st<u32>(M_TAG, tag);
    const float dtv = rbf(exp_f(p_ldt));
    const float alpha = -rbf(exp_f(p_lar)) * dtv, beta = p_aim * dtv;
    const float a2 = alpha * 1.44269504088896341f;
    const float th = beta * 0.15915494309189535f;
    const float tl = __builtin_fmaf(beta, 6.42063831672e-09f, __builtin_fmaf(beta, 0.15915494309189535f, -th));
    u32 mx = umax(umax(asu(p_cre) & 0x7fffffffu, asu(p_cim) & 0x7fffffffu), umax(asu(p_cre2) & 0x7fffffffu, asu(p_cim2) & 0x7fffffffu));
    mx = dpp_max<0xB1, 0xf>(mx); mx = dpp_max<0x4E, 0xf>(mx); mx = dpp_max<0x124, 0xf>(mx); mx = dpp_max<0x128, 0xf>(mx);
    mx = dpp_max<0x142, 0xa>(mx); mx = dpp_max<0x143, 0xc>(mx);
    int ex = (int)(__builtin_amdgcn_readlane(mx, 63) >> 23) + 1;
    ex = ex < 32 ? 32 : (ex > 230 ? 230 : ex);
    const float cscale = asf((u32)(254 + 14 - ex) << 23);
    const float yscale = asf((u32)(ex - 17) << 23) * dtv;
    cpx z8c = zpow(a2, th, tl, 8.f);
    z8c.i = -z8c.i;
    u32 tgb[4];
    UNROLL for (int j = 0; j < 4; ++j) tgb[j] = ((tag >> (6 * j)) & 7u) | (((tag >> (6 * j + 3)) & 7u) << 16);
    u32 an[57];
#define KEEP(I, Z) do { const cpx z_ = (Z); an[I] = pk_f16(z_.r, z_.i); PIN(an[I]); } while (0)
    cpx e0[16], e1[16];
    cpx y; y.r = p_cre * cscale; y.i = -p_cim * cscale;
    UNROLL for (int i = 0; i < YB0; ++i) { KEEP(i, y); y = cmul(y, z8c); }
    HTS(16);
    HBAR();
    {
      const u32 stv = lds_ld<u32>(M_ST + 4 * (l & 7));
      v4u o[2];
      UNROLL for (int k = 0; k < 2; ++k) o[k] = cat(lds_tr(lnr + 128 * k), lds_tr(lnr + 128 * k + 4 * 1088));
      SB();
      UNROLL for (int i = YB0; i < YB0 + 4; ++i) { KEEP(i, y); y = cmul(y, z8c); }
      SB();
      UNROLL for (int k = 0; k < 2; ++k) {
        UNROLL for (int j = 0; j < 4; ++j) o[k][j] = (o[k][j] & ~0x00070007u) | tgb[j];
        __builtin_amdgcn_raw_buffer_store_b128(o[k], rws, xno + 1024 * k, 0, AUX_SC1);
      }
      if (tid < 8) __hip_atomic_store((GAS u64*)(ws + WS_ST) + 16 * rb + tid, ((u64)tag << 32) | stv, RLX_AGENT);
      HTS(1);
      UNROLL for (int i = YB0 + 4; i < YB1; ++i) { KEEP(i, y); if (i < 15) y = cmul(y, z8c); }
    }
    HTS(17);
    NBAR();
    UNROLL for (int i = YB1; i < 16; ++i) { KEEP(i, y); if (i < 15) y = cmul(y, z8c); }
    const cpx z1 = zpow(a2, th, tl, 1.f);
    e0[0].r = 32768.f; e0[0].i = 0.f;
    e0[1].r = z1.r * 32768.f; e0[1].i = z1.i * 32768.f;
    UNROLL for (int i = 2; i < 8; ++i) e0[i] = cmul(e0[i - 1], z1);
    UNROLL for (int i = 0; i < 8; ++i) KEEP(16 + i, e0[i]);
    HTS(18);
    NBAR();
    const cpx z128 = zpow(a2, th, tl, 128.f);
    e0[8].r = z128.r * 32768.f; e0[8].i = z128.i * 32768.f;
    UNROLL for (int i = 9; i < 16; ++i) e0[i] = cmul(e0[i - 8], z128);
    UNROLL for (int i = 8; i < 16; ++i) KEEP(16 + i, e0[i]);
    const cpx z256 = cmul(z128, z128), z512 = cmul(z256, z256);
    UNROLL for (int i = 0; i < 2; ++i) { e1[i] = cmul(e0[8 * (i >> 2) + 2 * (i & 3)], z256); KEEP(32 + i, e1[i]); }
    HTS(19);
    NBAR();
    UNROLL for (int i = 2; i < 8; ++i) { e1[i] = cmul(e0[8 * (i >> 2) + 2 * (i & 3)], z256); KEEP(32 + i, e1[i]); }
    UNROLL for (int i = 0; i < 4; ++i) { e1[8 + i] = cmul(e0[8 * (i >> 1) + 4 * (i & 1)], z512); KEEP(40 + i, e1[8 + i]); }
    UNROLL for (int i = 0; i < 4; ++i) { e1[12 + i] = cmul(e1[4 * (i >> 1) + 2 * (i & 1)], z512); KEEP(44 + i, e1[12 + i]); }
    HTS(20);
    NBAR();
    {
      const cpx z1024 = zpow(a2, th, tl, 1024.f);
      cpx e2;
      e2.r = z1024.r * 32768.f; e2.i = z1024.i * 32768.f;
      KEEP(48, e2); KEEP(49, cmul(e0[8], z1024)); KEEP(50, cmul(e1[0], z1024)); KEEP(51, cmul(e1[4], z1024));
      KEEP(52, cmul(e1[8], z1024)); KEEP(53, cmul(e1[10], z1024)); KEEP(54, cmul(e1[12], z1024)); KEEP(55, cmul(e1[14], z1024));
      KEEP(56, cmul(e2, z1024));
    }
    v4u uv[2];
    UNROLL for (int i = 0; i < 2; ++i) uv[i] = __builtin_amdgcn_raw_buffer_load_b128(rws, uoff + i * 32 * 8192, 0, AUX_SC1);
    HTS(21);
    NBAR();
    __builtin_amdgcn_s_setprio(HPRIO);
    const u32 kx = H_S + (hw | 1) * 4352 + 16 * l;
    f32x4 kacc[3];
    {
      v4u yb[4], xf[3][4];
      if (EARLY == 0) NBAR();
      UNROLL for (int i = 0; i < 16; ++i) lds_st<u32>(sw[i >> 2] + 272 * (i & 3), an[i]);
      UNROLL for (int s = 0; s < 4; ++s) yb[s] = lds_ld<v4u>(sr + 64 * s);
      UNROLL for (int t = 0; t < 3; ++t) {
        if (EARLY == t + 1) NBAR();
        UNROLL for (int i = 0; i < (t < 2 ? 16 : 9); ++i) lds_st<u32>(sw[i >> 2] + 272 * (i & 3), an[16 + 16 * t + i]);
        UNROLL for (int s = 0; s < 4; ++s) xf[t][s] = lds_ld<v4u>(sr + 64 * s);
        if (t > 0) {
          SB();
          kacc[t - 1] = f32x4{0.f, 0.f, 0.f, 0.f};
          UNROLL for (int s = 0; s < 4; ++s) kacc[t - 1] = mfma16h(xf[t - 1][s], yb[s], kacc[t - 1]);
          SB();
        }
      }
      kacc[2] = f32x4{0.f, 0.f, 0.f, 0.f};
      UNROLL for (int s = 0; s < 4; ++s) kacc[2] = mfma16h(xf[2][s], yb[s], kacc[2]);
    }
    TS(24);
#define U_PUT() do { UNROLL for (int i = 0; i < 2; ++i) { \
      _Pragma("unroll 1") for (int it = 0; it < SPIN_CAP; ++it) { \
        UNROLL for (int j = 0; j < 4; ++j) uv[i][j] ^= tgb[j]; \
        if ((((uv[i].x | uv[i].y) | (uv[i].z | uv[i].w)) & 0x00070007u) == 0) break; \
        __builtin_amdgcn_s_sleep(PSLP); \
        FENCE(); \
        uv[i] = __builtin_amdgcn_raw_buffer_load_b128(rws, uoff + i * 32 * 8192, 0, AUX_SC1); \
      } \
      lds_st<v4u>(uw + 512 * i, uv[i]); } } while (0)
    const u32 uw = H_U + (l & 1) * 4608 + 512 + (64 * hw + (l >> 1)) * 16;
    if (hm) {
      UNROLL for (int i = 0; i < 3; ++i) lds_st<f32x4>(kx + 1024 * i, kacc[i]);
      rec_post(hrec, hw, 2);
      if (l < 32) lds_st<v4u>(H_U + chl * 4608 + 16 * l, z4);
      rec_wait1(hrec, hw ^ 1, 2);
    } else {
      rec_wait1(hrec, hw ^ 1, 2);
      u32 kp[3][2];
      UNROLL for (int i = 0; i < 3; ++i) {
        const f32x4 v = (kacc[i] + lds_ld<f32x4>(kx + 1024 * i)) * 4.76837158203125e-07f;
        kp[i][0] = pk_f16(v[0], v[1]); kp[i][1] = pk_f16(v[2], v[3]);
      }
      lds_st<v2u>(ta0, v2u{kp[0][0], kp[0][1]});
      lds_st<u32>(ta1, kp[1][0]); lds_st<u32>(ta1b, kp[1][1]);
      lds_st<u16>(ta2, (u16)(g < 3 ? kp[2][0] : 0u));
      if (g < 2) { lds_st<u16>(ta2 + 32, (u16)(kp[2][0] >> 16)); lds_st<u16>(ta2 + 64, (u16)kp[2][1]); lds_st<u16>(ta2 + 96, (u16)(kp[2][1] >> 16)); }
      rec_post(hrec, hw, 2);
    }
    TS(3);
    const u32 eb = H_S + chl * 8704;
    {
      u32 kv[17];
      UNROLL for (int e = 0; e < 17; ++e) kv[e] = lds_ld<u16>(xa[e]);
      SB();
      U_PUT();
      SB();
      v4u ea0, ea1, eb0, eb1;
      UNROLL for (int e = 0; e < 4; ++e) {
        ea0[e] = kv[2 * e] | (kv[2 * e + 1] << 16); ea1[e] = kv[2 * e + 8] | (kv[2 * e + 9] << 16);
        eb0[e] = kv[2 * e + 1] | (kv[2 * e + 2] << 16); eb1[e] = kv[2 * e + 9] | (kv[2 * e + 10] << 16);
      }
      const u32 a = eb + 32 * (64 * hm + l);
      lds_st<v4u>(a, ea0); lds_st<v4u>(a + 16, ea1); lds_st<v4u>(a + 4160, eb0); lds_st<v4u>(a + 4176, eb1);
      if (l < 4 && hm == 0) lds_st<v4u>(eb + 4096 + (l >> 1) * 4160 + 16 * (l & 1), z4);
    }
    rec_post(hrec, hw, 4);
    u32 fa4[4];
    {
      const int x0 = 2047 - 16 * (1 + 8 * hm) - i16 + 8 * g;
      const u32 a = eb + ((i16 & 1) ? 0 : 4160) + 4 * (x0 >> 1) - 512 * 7 - 192;
      UNROLL for (int i = 0; i < 4; ++i) { fa4[i] = a + 1024 * (3 - i); PIN(fa4[i]); }
    }
#define FA_LD(W, MI) cat(*(const LAS v2u_a4*)(uintptr_t)(fa4[(W) >> 1] + 512 * (1 - ((W) & 1)) + 192 - 64 * (MI)), \
                         *(const LAS v2u_a4*)(uintptr_t)(fa4[(W) >> 1] + 512 * (1 - ((W) & 1)) + 192 - 64 * (MI) + 8))
    v4u FA[4][8], G[4][8];
    TS(4);
    rec_wait4(hrec, 4);
    if (b == 0 && tid == 0) __hip_atomic_store((GAS u32*)(ws + WS_NONCE), tag, RLX_AGENT);
    TS(5);
    f32x4 acc[8];
    UNROLL for (int i = 0; i < 8; ++i) acc[i] = f32x4{0.f, 0.f, 0.f, 0.f};
    const u32 ub = H_U + chl * 4608 + 512;
    v2u uu[4];
    u32 pf;
    {
      const u32 a = ub + 32 * i16 + 16 * g - 32 * (1 + 8 * hm) - 192;
      G[0][0] = lds_ld<v4u>(a + 192);
      UNROLL for (int w = 0; w < 8; ++w) FA[0][w] = FA_LD(w, 0);
      UNROLL for (int v = 1; v < 8; ++v) G[0][v] = lds_ld<v4u>(a + 512 * v + 192);
      UNROLL for (int mi = 0; mi < 4; ++mi) {
        UNROLL for (int v = 0; v < 8; ++v) {
          SB();
          UNROLL for (int w = 0; v + w < 8; ++w) {
            acc[v + w] = mfma16h(FA[mi][w], G[mi][v], acc[v + w]);
            if (w == 0 && mi < 3) { SB(); G[mi + 1][v] = lds_ld<v4u>(a + 512 * v + 192 - 64 * (mi + 1)); SB(); }
            if (mi < 3 && v < 4 && (w == 1 || w == 3)) { SB(); FA[mi + 1][2 * v + (w >> 1)] = FA_LD(2 * v + (w >> 1), mi + 1); SB(); }
            if (mi == 3 && w == 0 && v < 4) { SB(); uu[v] = lds_ld<v2u>(ub + 2 * (256 * (4 * hm + v) + 16 * i16 + 4 * g)); SB(); }
            if (mi == 3 && w == 0 && v == 4) { SB(); pf = *(volatile LAS u32*)(uintptr_t)(hrec + 4 * (hw ^ 1)); SB(); }
          }
          if (mi == 2 && v == 4) rec_post(hrec, hw, 5);
        }
      }
    }
    TS(6);
    {
      const u32 xw = H_S + hw * 4352 + 16 * l, xrd = H_S + (hw ^ 1) * 4352 + 16 * l;
      if (!all64(pf >= 5)) rec_wait1(hrec, hw ^ 1, 5);
      FENCE();
      UNROLL for (int i = 0; i < 4; ++i) { const f32x4 lo = acc[i], hi = acc[4 + i]; lds_st<f32x4>(xw + 1024 * i, hm ? lo : hi); acc[i] = hm ? hi : lo; }
      rec_post(hrec, hw, 6);
      const float dsv = p_dsk * (1.0f / 1024.0f);
      u32 yo = WS_Y + ch * 4096 + hm * 2048 + 16 * l;
      PIN(yo);
      UNROLL for (int i = 0; i < 4; ++i) {
        const u32 ux = uu[i].x, uy = uu[i].y;
        const f16x2 u01 = __builtin_bit_cast(f16x2, ux), u23 = __builtin_bit_cast(f16x2, uy);
        acc[i][0] = __builtin_fmaf((float)u01.x, dsv, acc[i][0] * yscale); acc[i][1] = __builtin_fmaf((float)u01.y, dsv, acc[i][1] * yscale);
        acc[i][2] = __builtin_fmaf((float)u23.x, dsv, acc[i][2] * yscale); acc[i][3] = __builtin_fmaf((float)u23.y, dsv, acc[i][3] * yscale);
        PIN(acc[i]);
      }
      rec_wait1(hrec, hw ^ 1, 6);
      u32 o[4][2];
      UNROLL for (int i = 0; i < 4; ++i) {
        const f32x4 y = lds_ld<f32x4>(xrd + 1024 * i);
        o[i][0] = pk_bf16(__builtin_fmaf(y[0], yscale, acc[i][0]), __builtin_fmaf(y[1], yscale, acc[i][1]));
        o[i][1] = pk_bf16(__builtin_fmaf(y[2], yscale, acc[i][2]), __builtin_fmaf(y[3], yscale, acc[i][3]));
      }
      UNROLL for (int i = 0; i < 4; i += 2) {
        const auto s0 = __builtin_amdgcn_permlane16_swap(o[i][0], o[i + 1][0], false, false);
        const auto s1 = __builtin_amdgcn_permlane16_swap(o[i][1], o[i + 1][1], false, false);
        const v4u v = {s0[0], s1[0], s0[1], s1[1]};
        __builtin_amdgcn_raw_buffer_store_b128(v, rws, yo + (i >> 1) * 1024, 0, AUX_SC1);
      }
      rec_post(hrec, hw, 7);
      TS(2);
      WAIT_V(0);
      if (l == 0) __hip_atomic_store((GAS u32*)(ws + WS_FY) + FL * (4 * (c0 >> 1) + hw), tag, RLX_AGENT);
    }
    TS(7);
    xo = __builtin_amdgcn_raw_buffer_load_b128(RSRC(PTR(ka, 0), L * D * 2), ooff, 0, 0);
    if (hw == 0) lds_st<float>(M_RS + 4 * l, __builtin_amdgcn_rcpf(bf_hi(poll_granule((const GAS u64*)(ws + WS_ST) + 16 * (8 * tt + (l >> 3)) + (l & 7), tag))));
    poll_flag((const GAS u32*)(ws + WS_FY) + FL * (4 * (32 * dt + 8 * hw + (i16 >> 1)) + 2 * (l & 1) + (tt >> 4)), tag);
    AFTER_POLL();
    TS(8);
    {
      v4u yv[2];
      const int pc = l & 7;
      UNROLL for (int i = 0; i < 2; ++i)
        yv[i] = __builtin_amdgcn_raw_buffer_load_b128(rws, WS_Y + (64 * dt + 16 * hw + 8 * i + (l >> 3)) * 4096 + (tt >> 3) * 1024 +
                                                     (2 * (pc >> 2) + ((tt >> 2) & 1)) * 256 + (4 * (tt & 3) + (pc & 3)) * 16, 0, AUX_SC1);
      rec_wait4(hrec, 7);
      const u32 yst = H_S + hw * 4352;
      UNROLL for (int i = 0; i < 2; ++i) lds_st<v4u>(yst + (8 * i + (l >> 3)) * 192 + 32 * (pc & 3) + 16 * (pc >> 2), yv[i]);
      const u32 a = yst + (i16 >> 2) * 192 + 32 * g + 8 * (l & 3);
      const v4u y0 = cat(lds_tr(a), lds_tr(a + 4 * 192)), y1 = cat(lds_tr(a + 8 * 192), lds_tr(a + 12 * 192));
      lds_st<v4u>(H_U + l * 144 + 32 * hw, y0);
      lds_st<v4u>(H_U + l * 144 + 32 * hw + 16, y1);
    }
    TS(9);
  } else {
    __builtin_amdgcn_s_setprio(PRIO);
    const int w = wave - 4, wf = w & 1, wt = w >> 1;
    const int h = l >> 5, r32 = l & 31, blk = (l >> 4) & 1, q = (l & 15) >> 2, p = l & 3;
    const int mt = 4 * (xj >> 1) + (r & 3), nt = 7 * (xj & 1) + (r >> 2);
    u32 v_x[4], v_w1[4], v_l = 16 * l;
    UNROLL for (int i = 0; i < 4; ++i) {
      v_x[i] = (l >> 3) * 1024 + (((l & 7) ^ (l >> 4) ^ (4 * (i & 1))) * 16) + i * 7168;
      v_w1[i] = (l >> 4) * 3584 + (((l & 15) ^ ((l >> 4) << 2)) * 16) + i * (4 * 3584 - 1024);
      PIN(v_x[i]); PIN(v_w1[i]);
    }
    u32 s_x = (128 * mt + 32 * w) * 1024, s_w1 = (16 * w) * 3584 + 256 * nt, s_r = (256 * xj + 8 * r + 2 * w) * 1024;
    const int iu = 4 * (xj >> 1) + r - 28, ii = 14 * (xj & 1) + (iu < 14 ? iu : 0);
    asm volatile("" : "+s"(s_x), "+s"(s_w1), "+s"(s_r));
    PIN(v_l);
    ARGS_WAIT();
    TS(25);
    char* const ws = PTR(kc, 0);
    const v4i sx = {ka[0], ka[1], L * D * 2, 0x00020000}, sw1 = {ka[14], ka[15], D * F * 2, 0x00020000}, sws = {kc[0], kc[1], WS_END, 0x00020000};
    v4u xr[2];
    asm volatile("buffer_load_dwordx4 %0, %2, %3, %4 offen\n\tbuffer_load_dwordx4 %1, %2, %3, %4 offen offset:1024"
                 : "=&v"(xr[0]), "=&v"(xr[1]) : "v"(v_l), "s"(sx), "s"(s_r) : "memory");
    u32 so;
#define SET_M0(V) asm volatile("s_mov_b32 m0, %1" : "+s"(so) : "s"(V) : "memory")
#define GLDS(VO, SRD, I) asm volatile("buffer_load_dwordx4 %0, %1, %2 offen offset:%3 lds" ::"v"(VO), "s"(SRD), "s"(so), "n"((I) * 1024) : "memory")
#define SETG(N) do { const int G_ = (N) >> 2, T_ = G_ >> 1; \
        if (G_ & 1) { so = s_w1 + T_ * (64 * 3584); SET_M0((T_ & 3) * 32768 + 16384 + w * 4096); } \
        else { so = s_x + T_ * 128; SET_M0((T_ & 3) * 32768 + w * 4096); } } while (0)
#define GL(N) do { if (((N) >> 2) & 1) GLDS(v_w1[(N) & 3], sw1, (N) & 3); else GLDS(v_x[(N) & 3], sx, (N) & 3); } while (0)
#define GLRUN(N0, N1) do { UNROLL for (int n_ = (N0); n_ < (N1); ++n_) { if ((n_ & 3) == 0) { SETG(n_); asm volatile("s_nop 0"); } GL(n_); } } while (0)
    v4u wv[16];
    const u32 v_c = (l >> 3) * 3584 + (l & 7) * 16;
#define CSRUN(N0, N1) do { UNROLL for (int i_ = (N0); i_ < (N1); ++i_) { const u32 sof_ = (128 * w + 8 * i_) * 3584 + 128 * ii; \
        asm volatile("buffer_load_dwordx4 %0, %1, %2, %3 offen" : "=&v"(wv[i_]) : "v"(v_c), "s"(sw1), "s"(sof_) : "memory"); } } while (0)
    if (r < 28) GLRUN(0, NPRE); else CSRUN(0, NPRE);
    const __amdgpu_buffer_rsrc_t rws = RSRC(ws, WS_END), rw2 = RSRC(PTR(kb, 0), F * D * 2);
    const u32 v_w2 = (l >> 3) * 1024 + (l & 7) * 16;
    v4u wr[56];
#define RAW(I) wr[I] = __builtin_amdgcn_raw_buffer_load_b128(rw2, v_w2, (448 * w + 8 * (I)) * 1024 + 128 * dt, 0)
    u32 ab[2][4], aa[2][2];
    {
      const int row = 64 * wt + r32, z = (row >> 1) & 7;
      UNROLL for (int s = 0; s < 4; ++s) { ab[0][s] = row * 128 + (((2 * s + h) ^ z) * 16); ab[1][s] = ab[0][s] + 65536; PIN(ab[0][s]); PIN(ab[1][s]); }
      UNROLL for (int f2 = 0; f2 < 2; ++f2) {
        aa[0][f2] = 16384 + (8 * h + q) * 256 + (((8 * wf + 4 * f2 + 2 * blk + (p >> 1)) ^ (q << 2)) * 16) + 8 * (p & 1);
        aa[1][f2] = aa[0][f2] + 65536; PIN(aa[0][f2]); PIN(aa[1][f2]);
      }
    }
    u32 wa[2], lnw = H_U + 2 * w * 1088 + 16 * l;
    wa[0] = w * 8192 + (l >> 3) * 128 + (((l & 7) ^ (((l >> 4) & 1) << 2)) * 16); wa[1] = wa[0] + 65536;
    PIN(wa[0]); PIN(wa[1]); PIN(lnw);
#define W2_ST(I) lds_st<v4u>(wa[((I) >> 4) & 1] + (((I) >> 3) & 1) * 32768 + ((I) & 7) * 1024, wr[I])
    TS(9);
    asm volatile("s_waitcnt vmcnt(%2)" : "+v"(xr[0]), "+v"(xr[1]) : "n"(NPRE) : "memory");
    TS(12);
    {
      float s[2], qq[2], d[2][8], m[2], var[2];
      UNROLL for (int k = 0; k < 2; ++k) {
        s[k] = 0.f; qq[k] = 0.f;
        UNROLL for (int j = 0; j < 4; ++j) { const u32 u = xr[k][j]; s[k] = dot2(u, 0x3f803f80u, s[k]); qq[k] = dot2(u, u, qq[k]); }
      }
#define RED4(C, RM) do { s[0] = dpp_add<C, RM>(s[0]); qq[0] = dpp_add<C, RM>(qq[0]); s[1] = dpp_add<C, RM>(s[1]); qq[1] = dpp_add<C, RM>(qq[1]); } while (0)
      RED4(0xB1, 0xf); RED4(0x4E, 0xf); RED4(0x124, 0xf); RED4(0x128, 0xf); RED4(0x142, 0xa); RED4(0x143, 0xc);
      SB();
      if (r < 28) GLRUN(NPRE, NPRE + NM1); else CSRUN(NPRE, 16);
      SB();
      float e1[2];
      bool redo = false;
      UNROLL for (int k = 0; k < 2; ++k) {
        e1[k] = asf(__builtin_amdgcn_readlane(asu(s[k]), 63)) * (1.0f / D);
        const float e2 = asf(__builtin_amdgcn_readlane(asu(qq[k]), 63)) * (1.0f / D);
        var[k] = __builtin_fmaf(-e1[k], e1[k], e2);
        redo |= !(var[k] * 16.f >= e2);
        m[k] = rbf(e1[k]);
        UNROLL for (int j = 0; j < 4; ++j) { const u32 u = xr[k][j]; d[k][2 * j] = bf_lo(u) - m[k]; d[k][2 * j + 1] = bf_hi(u) - m[k]; }
      }
      if (__builtin_amdgcn_readfirstlane(redo)) {
        UNROLL for (int k = 0; k < 2; ++k) {
          s[k] = 0.f; qq[k] = 0.f;
          UNROLL for (int j = 0; j < 8; ++j) { s[k] += d[k][j]; qq[k] = __builtin_fmaf(d[k][j], d[k][j], qq[k]); }
        }
        RED4(0xB1, 0xf); RED4(0x4E, 0xf); RED4(0x124, 0xf); RED4(0x128, 0xf); RED4(0x142, 0xa); RED4(0x143, 0xc);
        UNROLL for (int k = 0; k < 2; ++k) {
          const float c1 = asf(__builtin_amdgcn_readlane(asu(s[k]), 63)) * (1.0f / D), c2 = asf(__builtin_amdgcn_readlane(asu(qq[k]), 63)) * (1.0f / D);
          var[k] = __builtin_fmaxf(__builtin_fmaf(-c1, c1, c2), 0.f);
        }
      }
      u32 pay[2];
      UNROLL for (int k = 0; k < 2; ++k) {
        const float sd = rbf(__builtin_amdgcn_sqrtf(rbf(rbf(var[k]) + 1e-5f)));
        const float rs = 1024.0f * __builtin_amdgcn_rcpf(sd);
        v4u o;
        UNROLL for (int j = 0; j < 4; ++j) {
          const u32 dd = pk_bf16(d[k][2 * j], d[k][2 * j + 1]);
          const u32 n = pk_bf16(bf_lo(dd) * rs, bf_hi(dd) * rs);
          o[j] = pk_f16(bf_lo(n), bf_hi(n));
        }
        lds_st<v4u>(lnw + k * 1088, o);
        pay[k] = (asu(m[k]) >> 16) | (asu(sd) & 0xffff0000u);
        if (k == 0) { SB(); if (r < 28) GLRUN(NPRE + NM1, NPRE + NM1 + NM2); SB(); }
      }
      lds_st<v2u>(M_ST + 8 * w, v2u{pay[0], pay[1]});
    }
    SB();
    TS(13);
    u32 tag;
    constexpr int NRAW0 = RAW_BEFORE(28) + NRL;
    constexpr int NJOIN = NRAW0 + NB;
    if (r < 28) {
      GLRUN(NPRE + NM1 + NM2, NPRO);
      const u32 crec = M_CF + 16 * l;
      f32x16 acc[2][2];
      UNROLL for (int i = 0; i < 2; ++i) UNROLL for (int j = 0; j < 2; ++j) UNROLL for (int e = 0; e < 16; ++e) acc[i][j][e] = 0.f;
      v4u fb[2][4][2];
      v2u fl[2][4][2], fh[2][4][2];
      v2u gm[2], gc[2];
#define RD(T, s_, j_) do { const int hi_ = ((T) >> 1) & 1, o_ = ((T) & 1) * 32768, u_ = (T) & 1; \
        if ((j_) == 0) fb[u_][s_][0] = lds_ld<v4u>(ab[hi_][s_] + o_); \
        if ((j_) == 1) fl[u_][s_][0] = lds_tr(aa[hi_][0] + o_ + (s_) * 4096); \
        if ((j_) == 2) fh[u_][s_][0] = lds_tr(aa[hi_][0] + o_ + (s_) * 4096 + 1024); \
        if ((j_) == 3) fb[u_][s_][1] = lds_ld<v4u>(ab[hi_][s_] + o_ + 4096); \
        if ((j_) == 4) fl[u_][s_][1] = lds_tr(aa[hi_][1] + o_ + (s_) * 4096); \
        if ((j_) == 5) fh[u_][s_][1] = lds_tr(aa[hi_][1] + o_ + (s_) * 4096 + 1024); } while (0)
#define RDN(T, J0, N) do { UNROLL for (int j2_ = (J0); j2_ < (J0) + (N); ++j2_) { \
        if ((T) < 7) RD(T, j2_ / 6, j2_ % 6); \
        else if (j2_ < 12) RD(T, j2_ / 3, j2_ % 3); else if (j2_ < 16) RD(T, (j2_ - 12) & 3, 3); else RD(T, ((j2_ - 16) >> 1) & 3, 4 + (j2_ & 1)); } } while (0)
#define MM(T, S, I, J) acc[I][J] = mfma32(cat(fl[(T) & 1][S][I], fh[(T) & 1][S][I]), fb[(T) & 1][S][J], acc[I][J])
#define SLOT(K, J) do { if ((K) < ND0) { GL(NPRO + 4 * (K) + 2 * (J)); GL(NPRO + 4 * (K) + 2 * (J) + 1); } \
        else if (IS_D(K)) GL(26 + 2 * (K) + (J)); else RAW(RAW_BEFORE(K) + (J)); } while (0)
#define NEXTG(K) do { if ((K) < ND0) SETG(NPRO + 4 * (K)); else if (IS_D(K) && ((K) & 1)) SETG(26 + 2 * (K)); } while (0)
      NEXTG(0);
      u32 v_g = r32 * 8, v_m = (r32 >> 3) * 128 + (r32 & 7) * 8, ho[2];
      UNROLL for (int j = 0; j < 2; ++j) { ho[j] = WS_H + ((4 * mt + 2 * wt + j) * 112 + 8 * nt + 4 * wf) * 1024 + 16 * l; PIN(ho[j]); }
      PIN(v_g); PIN(v_m);
      TS(16);
      PAD(2);
      BAR_V(NPRO - 8);
      TS(1);
      RDN(0, 0, 10);
      v4u ca[2], cb[2], cf;
      u32 tagv;
      SFOR(k, 0, 28)
        constexpr int t = k >> 2, s = k & 3;
        SB();
        MM(t, s, 0, 0);
        SB();
        if (s == 3) {
          if (t < 5) BAR_V(BEFORE(k) - LASTPOS(t + 1) - 1);
          if (t == 5) BAR_V(BEFORE(k) - LASTPOS(7) - 1);
          if (CTS || t == 3 || t == 5 || t == 6) TS(17 + t);
          RDN(t + 1, 0, 2);
        }
        SLOT(k, 0);
        SB();
        MM(t, s, 0, 1);
        SB();
        if (s == 3) RDN(t + 1, 2, 4);
        if (s == 0) RDN(t, 10, 4);
        if (s == 1) RDN(t, 17, 4);
        if (k == 2) tagv = lds_ld<u32>(M_TAG);
        SB();
        MM(t, s, 1, 0);
        SB();
        SLOT(k, 1);
        SB();
        MM(t, s, 1, 1);
        SB();
        if (s == 3) RDN(t + 1, 6, 4);
        if (s == 0) RDN(t, 14, 3);
        if (s == 1) RDN(t, 21, 3);
        if (k == KGRAN) {
          const u32 so_m = WS_ST + (16 * mt + 8 * wt) * 128, so_c = WS_CS + (128 * nt + 64 * wf) * 8;
          asm volatile("buffer_load_dwordx2 %0, %8, %5, %6 offen sc1\n\tbuffer_load_dwordx2 %1, %8, %5, %6 offen offset:512 sc1\n\t"
                       "buffer_load_dwordx2 %2, %4, %5, %7 offen sc1\n\tbuffer_load_dwordx2 %3, %4, %5, %7 offen offset:256 sc1"
                       : "=&v"(gm[0]), "=&v"(gm[1]), "=&v"(gc[0]), "=&v"(gc[1]) : "v"(v_g), "s"(sws), "s"(so_m), "s"(so_c), "v"(v_m) : "memory");
        }
        if (k == 6) tag = __builtin_amdgcn_readfirstlane(tagv);
        NEXTG(k + 1);
      SEND
      v4u ho_;
#define CVT(I, J, S, E) do { const s16x2 v_ = __builtin_bit_cast(s16x2, pk_bf16(acc[I][J][8 * (S) + 2 * (E)], acc[I][J][8 * (S) + 2 * (E) + 1])); \
        const s16x2 z_ = {0, 0}; ho_[E] = __builtin_bit_cast(u32, __builtin_elementwise_max(v_, z_)); } while (0)
#define HST(I, J, S) __builtin_amdgcn_raw_buffer_store_b128(ho_, rws, ho[J] + (2 * (I) + (S)) * 1024, 0, AUX_SC1)
      UNROLL for (int m = 0; m < 25; ++m) {
        const int a = m / 5, s = m % 5, pa_ = a - 1;
        SB();
        if (a < 4) { if (s < 4) MM(7, s & 3, (a >> 1) & 1, a & 1); else acc[(a >> 1) & 1][a & 1] = mfma32(ca[(a >> 1) & 1], cb[a & 1], acc[(a >> 1) & 1][a & 1]); }
        SB();
        if (m == 0) RDN(7, 10, 4);
        if (m == 1) {
          asm volatile("s_waitcnt vmcnt(%4)" : "+v"(gm[0]), "+v"(gm[1]), "+v"(gc[0]), "+v"(gc[1]) : "n"(BEFORE(28) - GPOS - 1 + (NRL > 0 ? 1 : 0)) : "memory");
          u32 bad = ((gm[0].y ^ tag) | (gm[1].y ^ tag)) | ((gc[0].y ^ tag) | (gc[1].y ^ tag));
          PIN(bad);
          if (__builtin_expect(__builtin_amdgcn_ballot_w64(bad != 0) != 0, 0)) {
            const GAS u64* pm = (const GAS u64*)(ws + WS_ST) + 16 * (16 * mt + 8 * wt + (r32 >> 3)) + (r32 & 7);
            const GAS u64* pc = (const GAS u64*)(ws + WS_CS) + 128 * nt + 64 * wf + r32;
#pragma unroll 1
            for (int it = 0; it < SPIN_CAP; ++it) {
              const u64 m0_ = __hip_atomic_load(pm, RLX_AGENT), m1_ = __hip_atomic_load(pm + 64, RLX_AGENT);
              const u64 c0_ = __hip_atomic_load(pc, RLX_AGENT), c1_ = __hip_atomic_load(pc + 32, RLX_AGENT);
              gm[0].x = (u32)m0_; gm[1].x = (u32)m1_; gc[0].x = (u32)c0_; gc[1].x = (u32)c1_;
              if (all64((u32)(m0_ >> 32) == tag && (u32)(m1_ >> 32) == tag && (u32)(c0_ >> 32) == tag && (u32)(c1_ >> 32) == tag)) break;
            }
          }
        }
        if (m == 2) { ca[0] = v4u{h ? 0u : gc[0].x, 0u, 0u, 0u}; PIN(ca[0]); }
        if (m == 2) { cb[0] = v4u{__builtin_amdgcn_perm(gm[0].x, gm[0].x, 0x01000100u) ^ 0x80008000u, 0u, 0u, 0u}; PIN(cb[0]); }
        if (m == 3) RDN(7, 14, 4);
        if (m == 4) { cb[1] = v4u{__builtin_amdgcn_perm(gm[1].x, gm[1].x, 0x01000100u) ^ 0x80008000u, 0u, 0u, 0u}; PIN(cb[1]); }
        if (m == 5) { RDN(7, 18, 6); rec_post(crec, w, 2); }
        if (m == 10) { ca[1] = v4u{h ? 0u : gc[1].x, 0u, 0u, 0u}; PIN(ca[1]); }
        if (m == 15) cf = *(volatile LAS v4u*)(uintptr_t)crec;
        if (m < NRL) RAW(m < NRL ? RAW_BEFORE(28) + m : 0);
        if (pa_ >= 0 && s >= 1) {
          const int I = (pa_ >> 1) & 1, J = pa_ & 1, S = (s - 1) >> 1, E = 2 * ((s - 1) & 1);
          CVT(I, J, S, E); CVT(I, J, S, E + 1);
          if (E == 2) HST(I, J, S);
        }
      }
      static_assert(NRL <= 7 && NJOIN <= 56 - NFILL && NJOIN >= 28 && NRAW0 <= 32, "w2 schedule");
      SB();
      PAD(5);
      TS(10);
      if (!all64(umin(umin(cf.x, cf.y), umin(cf.z, cf.w)) >= 2u)) rec_wait4(crec, 2);
      FENCE();
      UNROLL for (int i = 0; i < NRAW0; ++i) W2_ST(i);
      SB();
      UNROLL for (int i = NRAW0; i < NJOIN; ++i) RAW(i);
      SB();
      TS(3);
      WAIT_V(NB);
      if (l == 0) __hip_atomic_store((GAS u32*)(ws + WS_FH) + FL * ((2 * mt + wt) * 32 + 2 * nt + wf), tag, RLX_AGENT);
      TS(4);
    } else {
      __builtin_amdgcn_s_setprio(0);
      u32 tch[7];
      const u32 v_t = (l >> 2) * 1024 + (l & 3) * 128;
#define TOUCH(I) tch[I] = __builtin_amdgcn_raw_buffer_load_b32(rw2, v_t, (448 * (r - 28) + 64 * (I) + 16 * w) * 1024, 0)
      TOUCH(0); RAW(0); RAW(1); RAW(2); RAW(3);
      HBAR();
      asm volatile("s_waitcnt vmcnt(5)" : "+v"(wv[0]), "+v"(wv[1]), "+v"(wv[2]), "+v"(wv[3]), "+v"(wv[4]), "+v"(wv[5]), "+v"(wv[6]), "+v"(wv[7]),
                   "+v"(wv[8]), "+v"(wv[9]), "+v"(wv[10]), "+v"(wv[11]), "+v"(wv[12]), "+v"(wv[13]), "+v"(wv[14]), "+v"(wv[15])::"memory");
      TOUCH(1); RAW(4); RAW(5); RAW(6); RAW(7);
      SB();
      float cs[8];
      u32 one_lo = 0x00003f80u, one_hi = 0x3f800000u;
      PIN(one_lo); PIN(one_hi);
      UNROLL for (int j = 0; j < 8; ++j) cs[j] = 0.f;
      UNROLL for (int i = 0; i < 16; ++i) UNROLL for (int j = 0; j < 4; ++j) { const u32 u = wv[i][j]; cs[2 * j] = dot2(u, one_lo, cs[2 * j]); cs[2 * j + 1] = dot2(u, one_hi, cs[2 * j + 1]); }
      lds_st<f32x4>(98304 + (8 * w + (l >> 3)) * 272 + 32 * (l & 7), f32x4{cs[0], cs[1], cs[2], cs[3]});
      lds_st<f32x4>(98304 + (8 * w + (l >> 3)) * 272 + 32 * (l & 7) + 16, f32x4{cs[4], cs[5], cs[6], cs[7]});
      HBAR();
      tag = __builtin_amdgcn_readfirstlane(lds_ld<u32>(M_TAG));
      {
        float s = 0.f;
        UNROLL for (int i = 0; i < 8; ++i) s += lds_ld<float>(98304 + (8 * (l >> 4) + i) * 272 + 4 * (16 * w + (l & 15)));
        const auto s16 = __builtin_amdgcn_permlane16_swap(asu(s), asu(s), false, false);
        s = asf(s16[0]) + asf(s16[1]);
        const auto s32 = __builtin_amdgcn_permlane32_swap(asu(s), asu(s), false, false);
        s = asf(s32[0]) + asf(s32[1]);
        const float hi = rbf(s);
        if (iu < 14 && l < 16) __hip_atomic_store((GAS u64*)(ws + WS_CS) + 64 * ii + 16 * w + l, ((u64)tag << 32) | pk_bf16(hi, s - hi), RLX_AGENT);
      }
      TS(2);
      UNROLL for (int i = 2; i < 7; ++i) { TOUCH(i); RAW(4 * i); RAW(4 * i + 1); RAW(4 * i + 2); RAW(4 * i + 3); NBAR(); }
      UNROLL for (int i = 0; i < NRAW0; ++i) { if (28 + i < NJOIN) RAW(28 + i < NJOIN ? 28 + i : 0); SB(); W2_ST(i); SB(); }
      UNROLL for (int i = 0; i < 7; ++i) asm volatile("" ::"v"(tch[i]));
      __builtin_amdgcn_s_waitcnt(0x0F70);
      SB();
    }
    u32 a2[2][2];
    UNROLL for (int d2 = 0; d2 < 2; ++d2) {
      a2[0][d2] = w * 8192 + (4 * h + q) * 128 + (((4 * d2 + 2 * blk + (p >> 1)) ^ (((q >> 1) & 1) << 2)) * 16) + 8 * (p & 1);
      a2[1][d2] = a2[0][d2] + 65536; PIN(a2[0][d2]); PIN(a2[1][d2]);
    }
#define W2_FRAG(K, D2) cat(lds_tr(a2[((K) >> 3) & 1][D2] + (((K) >> 2) & 1) * 32768 + ((K) & 3) * 2048), lds_tr(a2[((K) >> 3) & 1][D2] + (((K) >> 2) & 1) * 32768 + ((K) & 3) * 2048 + 1024))
    UNROLL for (int i = NJOIN; i < 56 - NFILL; ++i) { RAW(i); SB(); if (i >= NJOIN + 4 && i - NJOIN - 4 + NRAW0 < 32) { W2_ST(i - NJOIN - 4 + NRAW0); SB(); } }
    static_assert(56 - NFILL - NJOIN - 4 + NRAW0 >= 32, "w2 schedule");
    TS(5);
    const GAS u32* fhp = (const GAS u32*)(ws + WS_FH) + FL * (tt * 32 + 7 * w + (l < 7 ? l : 0));
    const u32 fh0 = __hip_atomic_load(fhp, RLX_AGENT);
    SB();
    UNROLL for (int i = 56 - NFILL; i < 56; ++i) RAW(i);
    SB();
    v4u fa[28][2];
    UNROLL for (int k = 0; k < 2; ++k) { fa[k][0] = W2_FRAG(k, 0); fa[k][1] = W2_FRAG(k, 1); }
    u32 s_h = WS_H + ((2 * tt) * 112 + 28 * w) * 1024;
    u32 v_h[2] = {(u32)(16 * l), (u32)(16 * l + 112 * 1024)};
    asm volatile("" : "+s"(s_h));
    PIN(v_h[0]); PIN(v_h[1]);
    SB();
    TS(11);
    PAD(3);
    if (!all64(fh0 == tag)) poll_flag(fhp, tag);
    AFTER_POLL();
    TS(6);
    f32x16 acc[2][2];
    UNROLL for (int i = 0; i < 2; ++i) UNROLL for (int j = 0; j < 2; ++j) UNROLL for (int e = 0; e < 16; ++e) acc[i][j][e] = 0.f;
    v4u hb[28][2];
#define H_LOAD(K, T2) hb[K][T2] = __builtin_amdgcn_raw_buffer_load_b128(rws, v_h[T2] + ((K) & 3) * 1024, s_h + ((K) >> 2) * 4096, AUX_SC1)
    UNROLL for (int k = 0; k < HWIN; ++k) { H_LOAD(k, 0); SB(); H_LOAD(k, 1); SB(); }
    SB();
    UNROLL for (int k = 0; k < 24; ++k) {
      const int kn = k + 2, st = 32 + 2 * (k - 2);
      acc[0][0] = mfma32(fa[k][0], hb[k][0], acc[0][0]);
      SB();
      fa[kn][0] = W2_FRAG(kn, 0);
      if (k >= 22) fa[kn + 2][0] = W2_FRAG(kn + 2, 0);
      SB();
      acc[1][0] = mfma32(fa[k][1], hb[k][0], acc[1][0]);
      SB();
      if (k + HWIN < 28) H_LOAD(k + HWIN, 0);
      if (k >= 2 && k < 14) W2_ST(k >= 2 && k < 14 ? st : 32);
      SB();
      acc[0][1] = mfma32(fa[k][0], hb[k][1], acc[0][1]);
      SB();
      fa[kn][1] = W2_FRAG(kn, 1);
      if (k >= 22) fa[kn + 2][1] = W2_FRAG(kn + 2, 1);
      SB();
      acc[1][1] = mfma32(fa[k][1], hb[k][1], acc[1][1]);
      SB();
      if (k + HWIN < 28) H_LOAD(k + HWIN, 1);
      if (k >= 2 && k < 14) W2_ST(k >= 2 && k < 14 ? st + 1 : 32);
      if (k == 0) TS(26);

      if (k == 14) xo = __builtin_amdgcn_raw_buffer_load_b128(RSRC(PTR(ka, 0), L * D * 2), ooff, 0, 0);
      SB();
    }
    TS(7);
    u32 pw[2][2];
    UNROLL for (int i = 0; i < 2; ++i) UNROLL for (int e = 0; e < 2; ++e) { pw[i][e] = w * 8192 + r32 * 256 + (((4 * i + 2 * e + h) ^ (r32 & 7)) * 16); PIN(pw[i][e]); }
    UNROLL for (int m = 0; m < 21; ++m) {
      const int a = m >> 2, mp = m - 5, pa_ = mp >> 2, e = mp & 3;
      SB();
      if (a < 4) acc[a & 1][(a >> 1) & 1] = mfma32(fa[24 + (m & 3)][a & 1], hb[24 + (m & 3)][(a >> 1) & 1], acc[a & 1][(a >> 1) & 1]);
      SB();
      if (mp >= 0) {
        const int i = pa_ & 1, j = (pa_ >> 1) & 1;
        const f32x4 v = {acc[i][j][4 * e], acc[i][j][4 * e + 1], acc[i][j][4 * e + 2], acc[i][j][4 * e + 3]};
        lds_st<f32x4>(pw[e >> 1][e & 1] + j * 32768 + i * 128, v);
      }
    }
    TS(8);
  }
  PAD(4);
  BAR_L();
  TS(14);
  {
    const int tok = tid >> 3, o = tid & 7;
    const u32 a = (tok >> 5) * 32768 + (tok & 31) * 256;
    const u32 c0a = a + (((2 * o) ^ (tok & 7)) * 16), c1a = a + (((2 * o + 1) ^ (tok & 7)) * 16);
    f32x4 s0 = (lds_ld<f32x4>(c0a) + lds_ld<f32x4>(c0a + 8192)) + (lds_ld<f32x4>(c0a + 16384) + lds_ld<f32x4>(c0a + 24576));
    f32x4 s1 = (lds_ld<f32x4>(c1a) + lds_ld<f32x4>(c1a + 8192)) + (lds_ld<f32x4>(c1a + 16384) + lds_ld<f32x4>(c1a + 24576));
    const float rs = lds_ld<float>(M_RS + 4 * tok);
    const v4u R = lds_ld<v4u>(H_U + tok * 144 + 16 * o);
    v4u res;
    UNROLL for (int j = 0; j < 4; ++j) {
      const u32 xj_ = xo[j], rj = R[j];
      const float bl = j < 2 ? s0[2 * (j & 1)] : s1[2 * (j & 1)], bh = j < 2 ? s0[2 * (j & 1) + 1] : s1[2 * (j & 1) + 1];
      res[j] = pk_bf16(__builtin_fmaf(bl, rs, bf_lo(xj_) + bf_lo(rj)), __builtin_fmaf(bh, rs, bf_hi(xj_) + bf_hi(rj)));
    }
    __builtin_amdgcn_raw_buffer_store_b128(res, RSRC(PTR(kb, 1), L * D * 2), ooff, 0, AUX_SC1);
  }
  PAD(0);
}

extern "C" void kernel_launch(void* const* d_in, const int* in_sizes, int n_in, void* d_out, int out_size, void* d_ws, size_t ws_size,
                              hipStream_t stream) {
  fused_s4d_mlp<<<256, 512, 0, stream>>>((const u16*)d_in[0], (const u16*)d_in[1], (const u16*)d_in[2], (const u16*)d_in[3], (const u16*)d_in[4],
                                         (const u16*)d_in[5], (const u16*)d_in[6], (const u16*)d_in[8], (const u16*)d_in[9], (u16*)d_out,
                                         (char*)d_ws);
}
```

```cpp
#include <hip/hip_runtime.h>
#include <stdint.h>

typedef unsigned short u16;
typedef unsigned u32;
typedef unsigned long long u64;
typedef u32 v4u __attribute__((ext_vector_type(4)));
typedef u32 v2u __attribute__((ext_vector_type(2)));
typedef u32 v2u_a4 __attribute__((ext_vector_type(2), aligned(4)));
typedef int v4i __attribute__((ext_vector_type(4)));
typedef int v2i __attribute__((ext_vector_type(2)));
typedef int v16i __attribute__((ext_vector_type(16)));
typedef short v4s __attribute__((ext_vector_type(4)));
typedef float f32x2 __attribute__((ext_vector_type(2)));
typedef float f32x4 __attribute__((ext_vector_type(4)));
typedef float f32x16 __attribute__((ext_vector_type(16)));
typedef __bf16 bf16x2 __attribute__((ext_vector_type(2)));
typedef __bf16 bf16x8 __attribute__((ext_vector_type(8)));
typedef _Float16 f16x2 __attribute__((ext_vector_type(2)));
typedef _Float16 f16x8 __attribute__((ext_vector_type(8)));
typedef short s16x2 __attribute__((ext_vector_type(2)));
#define LAS __attribute__((address_space(3)))
#define GAS __attribute__((address_space(1)))
#if defined(__HIP_DEVICE_COMPILE__)
#define NOPK __attribute__((target("no-packed-fp32-ops")))
#else
#define NOPK
#endif
#define DI NOPK __device__ __forceinline__
#define RLX_AGENT __ATOMIC_RELAXED, __HIP_MEMORY_SCOPE_AGENT
#define SB() __builtin_amdgcn_sched_barrier(0)
#define PIN(x) asm volatile("" : "+v"(x))
#define UNROLL _Pragma("unroll")

#define DBG 0
#define NPRO 16
#define NR0 0
#define NRAMP 4
#define GAP0 4
#define GAP1 7
#define RMAX 22
#define DMAX 64
#define XMASK 0
#define NL 0
#define KGRAN 22
#define PRIO 2
#define PSLP 4
#define NB 8
#define NFILL 0
#define HPRIO 1
#define HPRIO2 3
#define KP1 10
#define KP2 14
#define HGROW 8
#define HADD 1
#define PDLY 0
#define MAP2 1
#define XNL 0
#define HWIN 5
#define PRE 0
#define LNB 2
#define XAPOS 1
#define GPOSN 2
#define BARS 0xfd0
#define LNPRIO 3

struct Sched {
  int nd[28], d0[29], r0[29], nx[28];
  int before[29];
  int last[12];
  int gpos;
};
constexpr Sched make_sched() {
  Sched S{};
  int n = NPRO, rw = NR0, cnt = NPRO + NR0;
  for (int i = 0; i < NPRO; ++i) S.last[i >> 3] = i;
  for (int k = 0; k < 28; ++k) {
    S.before[k] = cnt; S.d0[k] = n; S.r0[k] = rw;
    const int lim = 32 + 8 * ((k + 1) >> 2), want = k < NRAMP ? 4 : 2;
    if (!(k >= GAP0 && k < GAP1) && n + want <= (lim < DMAX ? lim : DMAX)) {
      S.nd[k] = want;
      for (int j = 0; j < want; ++j) { S.last[n >> 3] = cnt; ++n; ++cnt; }
    } else { S.nd[k] = 0; if (rw + 2 <= RMAX) { rw += 2; cnt += 2; } else S.nd[k] = -1; }
    S.nx[k] = ((XMASK >> k) & 1) && rw < RMAX;
    if (S.nx[k]) { ++rw; ++cnt; }
    if (k == KGRAN) { cnt += 4; S.gpos = cnt - 1; }
  }
  S.before[28] = cnt; S.r0[28] = rw; S.d0[28] = n;
  return S;
}
constexpr Sched SC = make_sched();
constexpr bool sched_ok() {
  for (int t = 0; t < 7; ++t) if (SC.d0[4 * t + 3] < 8 * (t + 2)) return false;
  for (int k = 0; k < 28; ++k) if (SC.nd[k] == 4 && (SC.d0[k] & 3)) return false;
  return (NPRO & 3) == 0 && NPRO >= 8 && KGRAN < 27;
}
static_assert(sched_ok(), "load schedule");
#define MIN_(A, B) ((A) < (B) ? (A) : (B))

constexpr int L = 2048, D = 512, NS = 128, F = 1792;
constexpr int WS_NONCE = 0;
constexpr int FL = 32;
constexpr int WS_CS = 16384;
constexpr int WS_ST = 32768;
constexpr int WS_FH = 65536;
constexpr int WS_FY = WS_FH + 131072;
constexpr int WS_XN = 524288;
constexpr int WS_Y = WS_XN + L * D * 2;
constexpr int WS_H = WS_Y + L * D * 2;
constexpr int WS_END = WS_H + L * F * 2;
constexpr int WS_DBG = 16 << 20;
constexpr int HB = 131072;
constexpr int H_U = HB;
constexpr int H_S = H_U + 9216;
constexpr int H_T = H_S + 16640;
constexpr int M_CF = H_T + 5280;
constexpr int M_HF = M_CF + 256;
constexpr int M_ONE = M_HF + 256;
constexpr int M_RS = M_ONE + 512;
constexpr int M_ST = M_RS + 256;
static_assert(M_ST + 32 <= 163840 && 4 * 6256 <= H_T - H_U, "LDS map");
constexpr int LDS_BYTES = 163840;
constexpr int SPIN_CAP = 1 << 16;
constexpr int AUX_SC1 = 16;

DI float asf(u32 v) { return __builtin_bit_cast(float, v); }
DI u32 asu(float v) { return __builtin_bit_cast(u32, v); }
DI float bf_lo(u32 v) { return asf(v << 16); }
DI float bf_hi(u32 v) { return asf(v & 0xffff0000u); }
DI u32 pk_bf16(float lo, float hi) { f32x2 v = {lo, hi}; return __builtin_bit_cast(u32, __builtin_convertvector(v, bf16x2)); }
DI float rbf(float f) { return bf_lo(pk_bf16(f, f)); }
DI u32 pk_f16(float lo, float hi) { f32x2 v = {lo, hi}; return __builtin_bit_cast(u32, __builtin_convertvector(v, f16x2)); }
DI f32x16 mfma32(v4u a, v4u b, f32x16 c) { return __builtin_amdgcn_mfma_f32_32x32x16_bf16(__builtin_bit_cast(bf16x8, a), __builtin_bit_cast(bf16x8, b), c, 0, 0, 0); }
DI f32x4 mfma16h(v4u a, v4u b, f32x4 c) { return __builtin_amdgcn_mfma_f32_16x16x32_f16(__builtin_bit_cast(f16x8, a), __builtin_bit_cast(f16x8, b), c, 0, 0, 0); }
template <class T> DI T lds_ld(u32 a) { return *(const LAS T*)(uintptr_t)a; }
template <class T> DI void lds_st(u32 a, T v) { *(LAS T*)(uintptr_t)a = v; }
DI v2u lds_tr(u32 a) { return __builtin_bit_cast(v2u, __builtin_amdgcn_ds_read_tr16_b64_v4i16((LAS v4s*)(uintptr_t)a)); }
DI v4u cat(v2u a, v2u b) { v4u r = {a.x, a.y, b.x, b.y}; return r; }
DI bool all64(bool c) { return __builtin_amdgcn_ballot_w64(c) == ~0ull; }
DI u32 umin(u32 a, u32 b) { return a < b ? a : b; }
DI u32 umax(u32 a, u32 b) { return a > b ? a : b; }
template <int CTRL, int RM> DI float dpp_add(float v) { return v + asf(__builtin_amdgcn_update_dpp(0, asu(v), CTRL, RM, 0xf, true)); }
template <int CTRL, int RM> DI u32 dpp_max(u32 v) { return umax(v, (u32)__builtin_amdgcn_update_dpp(0, (int)v, CTRL, RM, 0xf, true)); }
#define BAR_L() asm volatile("s_waitcnt lgkmcnt(0)\n\ts_barrier" ::: "memory")
#define BAR_V(N) asm volatile("s_waitcnt vmcnt(%0) lgkmcnt(0)\n\ts_barrier" ::"n"(N) : "memory")
#define HBAR() do { SB(); BAR_L(); SB(); } while (0)
#define NBAR() do { SB(); asm volatile("s_barrier" ::: "memory"); SB(); } while (0)
#define WAIT_V(N) asm volatile("s_waitcnt vmcnt(%0)" ::"n"(N) : "memory")
#define FENCE() asm volatile("" ::: "memory")
DI void rec_post(u32 own, u32 v) { FENCE(); *(volatile LAS u32*)(uintptr_t)own = v; FENCE(); }
DI u32 rec_peek(u32 a) { return *(volatile LAS u32*)(uintptr_t)a; }
DI void rec_spin(u32 a, u32 v) {
  u32 t, n = SPIN_CAP * 8;
  asm volatile("1:\n\tds_read_b32 %0, %2\n\ts_sub_u32 %1, %1, 1\n\ts_cbranch_scc1 2f\n\ts_waitcnt lgkmcnt(0)\n\tv_cmp_gt_u32 vcc, %3, %0\n\ts_cbranch_vccnz 1b\n2:\n\ts_waitcnt lgkmcnt(0)"
               : "=&v"(t), "+s"(n) : "v"(a), "n"(v) : "memory", "vcc", "scc");
}
template <int SL> DI void rec_wait(u32 a, u32 v) {
#pragma unroll 1
  for (int i = 0; i < SPIN_CAP * 8; ++i) {
    if (all64(rec_peek(a) >= v)) break;
    __builtin_amdgcn_s_sleep(SL);
  }
  FENCE();
}
#define REC_WAIT(A, V) REC_CHECK(rec_peek(A), A, V)
#define REC_CHECK(F, A, V) do { if (__builtin_expect(__builtin_amdgcn_ballot_w64((F) < (u32)(V)) != 0, 0)) rec_wait<0>(A, V); FENCE(); } while (0)
DI void poll_flag(const GAS u32* f, u32 tag) {
#pragma unroll 1
  for (int i = 0; i < SPIN_CAP; ++i) {
    const u32 v = __hip_atomic_load(f, RLX_AGENT);
    if (all64(v == tag)) break;
    __builtin_amdgcn_s_sleep(PSLP);
  }
}
#define AFTER_POLL() do { __builtin_amdgcn_fence(__ATOMIC_ACQUIRE, "wavefront"); FENCE(); } while (0)
DI u32 poll_granule(const GAS u64* g, u32 tag) {
  u64 v = 0;
#pragma unroll 1
  for (int i = 0; i < SPIN_CAP; ++i) {
    v = __hip_atomic_load(g, RLX_AGENT);
    if (all64((u32)(v >> 32) == tag)) break;
    __builtin_amdgcn_s_sleep(1);
  }
  return (u32)v;
}
DI float dot2(u32 a, u32 b, float c) { return __builtin_amdgcn_fdot2_f32_bf16(__builtin_bit_cast(bf16x2, a), __builtin_bit_cast(bf16x2, b), c, false); }
template <int V> struct ic { static constexpr int v = V; };
template <int I, int N, class Fn> DI void static_for(Fn&& f) { if constexpr (I < N) { f(ic<I>{}); static_for<I + 1, N>(f); } }
#define SFOR(VAR, N0, N1) static_for<N0, N1>([&](auto VAR##_) NOPK __attribute__((always_inline)) { constexpr int VAR = decltype(VAR##_)::v;
#define SEND });
struct cpx { float r, i; };
DI cpx cmul(cpx a, cpx b) { cpx c; c.r = __builtin_fmaf(a.r, b.r, -(a.i * b.i)); c.i = __builtin_fmaf(a.r, b.i, a.i * b.r); return c; }
DI v4u cmulh(v4u a, v4u b) {
  u32 c0, c1, c2, c3;
  const u32 a0 = a.x, a1 = a.y, a2 = a.z, a3 = a.w, b0 = b.x, b1 = b.y, b2 = b.z, b3 = b.w;
  asm("v_pk_mul_f16 %0, %4, %8 op_sel_hi:[0,1]\n\tv_pk_mul_f16 %1, %5, %9 op_sel_hi:[0,1]\n\t"
      "v_pk_mul_f16 %2, %6, %10 op_sel_hi:[0,1]\n\tv_pk_mul_f16 %3, %7, %11 op_sel_hi:[0,1]\n\t"
      "v_pk_fma_f16 %0, %4, %8, %0 op_sel:[1,1,0] op_sel_hi:[1,0,1] neg_lo:[0,1,0]\n\t"
      "v_pk_fma_f16 %1, %5, %9, %1 op_sel:[1,1,0] op_sel_hi:[1,0,1] neg_lo:[0,1,0]\n\t"
      "v_pk_fma_f16 %2, %6, %10, %2 op_sel:[1,1,0] op_sel_hi:[1,0,1] neg_lo:[0,1,0]\n\t"
      "v_pk_fma_f16 %3, %7, %11, %3 op_sel:[1,1,0] op_sel_hi:[1,0,1] neg_lo:[0,1,0]"
      : "=&v"(c0), "=&v"(c1), "=&v"(c2), "=&v"(c3) : "v"(a0), "v"(a1), "v"(a2), "v"(a3), "v"(b0), "v"(b1), "v"(b2), "v"(b3));
  return v4u{c0, c1, c2, c3};
}
DI cpx zpow(float a2, float th, float tl, float p) {
  const float mag = __builtin_amdgcn_exp2f(a2 * p);
  const float ph = __builtin_fmaf(tl, p, __builtin_amdgcn_fractf(th * p));
  cpx z; z.r = mag * __builtin_amdgcn_cosf(ph); z.i = mag * __builtin_amdgcn_sinf(ph); return z;
}
DI float exp_f(float x) {
  const float t = x * 1.44269502162933349609375f;
  const float e = __builtin_fmaf(x, 1.925963033500011079e-08f, __builtin_fmaf(x, 1.44269502162933349609375f, -t));
  const float r = __builtin_amdgcn_exp2f(t);
  const float c = __builtin_fmaf(r * e, 0.693147180559945f, r);
  return c == c ? c : r;
}

template <class Fn> DI void ln_rows(const v4u (&xr)[2], u32 img, u32 stat, Fn&& tags) {
  float s[2], q[2], d[2][8], m[2], var[2];
  UNROLL for (int k = 0; k < 2; ++k) {
    s[k] = 0.f; q[k] = 0.f;
    UNROLL for (int j = 0; j < 4; ++j) { const u32 u = xr[k][j]; s[k] = dot2(u, 0x3f803f80u, s[k]); q[k] = dot2(u, u, q[k]); }
  }
#define RED4(C, RM) do { s[0] = dpp_add<C, RM>(s[0]); q[0] = dpp_add<C, RM>(q[0]); s[1] = dpp_add<C, RM>(s[1]); q[1] = dpp_add<C, RM>(q[1]); } while (0)
#define RED4_ALL() do { RED4(0xB1, 0xf); RED4(0x4E, 0xf); RED4(0x124, 0xf); RED4(0x128, 0xf); RED4(0x142, 0xa); RED4(0x143, 0xc); } while (0)
  RED4_ALL();
  u32 one_lo = 0x00003f80u, one_hi = 0x3f800000u;
  asm volatile("" : "+s"(one_lo), "+s"(one_hi));
  bool redo = false;
  UNROLL for (int k = 0; k < 2; ++k) {
    const float e1 = asf(__builtin_amdgcn_readlane(asu(s[k]), 63)) * (1.0f / D), e2 = asf(__builtin_amdgcn_readlane(asu(q[k]), 63)) * (1.0f / D);
    var[k] = __builtin_fmaf(-e1, e1, e2);
    redo |= !(var[k] * 16.f >= e2);
    m[k] = rbf(e1);
    float nm = -m[k];
    PIN(nm);
    UNROLL for (int j = 0; j < 4; ++j) {
      const u32 u = xr[k][j];
      asm("v_dot2_f32_bf16 %0, %2, %3, %5\n\tv_dot2_f32_bf16 %1, %2, %4, %5" : "=&v"(d[k][2 * j]), "=&v"(d[k][2 * j + 1]) : "v"(u), "s"(one_lo), "s"(one_hi), "v"(nm));
    }
  }
  asm volatile("s_nop 2" : "+v"(d[0][0]), "+v"(d[0][1]), "+v"(d[0][2]), "+v"(d[0][3]), "+v"(d[0][4]), "+v"(d[0][5]), "+v"(d[0][6]), "+v"(d[0][7]),
               "+v"(d[1][0]), "+v"(d[1][1]), "+v"(d[1][2]), "+v"(d[1][3]), "+v"(d[1][4]), "+v"(d[1][5]), "+v"(d[1][6]), "+v"(d[1][7]));
  SB();
  if (__builtin_expect(__builtin_amdgcn_ballot_w64(redo) != 0, 0)) {
    UNROLL for (int k = 0; k < 2; ++k) {
      s[k] = 0.f; q[k] = 0.f;
      UNROLL for (int j = 0; j < 8; ++j) { s[k] += d[k][j]; q[k] = __builtin_fmaf(d[k][j], d[k][j], q[k]); }
    }
    RED4_ALL();
    UNROLL for (int k = 0; k < 2; ++k) {
      const float c1 = asf(__builtin_amdgcn_readlane(asu(s[k]), 63)) * (1.0f / D), c2 = asf(__builtin_amdgcn_readlane(asu(q[k]), 63)) * (1.0f / D);
      var[k] = __builtin_fmaxf(__builtin_fmaf(-c1, c1, c2), 0.f);
    }
  }
  const u32 tg = tags();
  u32 msk = ~0x00070007u;
  PIN(msk);
  u32 pay[2];
  UNROLL for (int k = 0; k < 2; ++k) {
    const float sd = rbf(__builtin_amdgcn_sqrtf(rbf(rbf(var[k]) + 1e-5f)));
    const float rs = 1024.0f * __builtin_amdgcn_rcpf(sd);
    v4u o;
    const u32 tr = (k ? tg >> 16 : tg & 7u) * 0x10001u;
    UNROLL for (int j = 0; j < 4; ++j) o[j] = ((pk_f16(d[k][2 * j] * rs, d[k][2 * j + 1] * rs) + 0x00040004u) & msk) | tr;
    lds_st<v4u>(img + k * 1088, o);
    pay[k] = (asu(m[k]) >> 16) | (asu(sd) & 0xffff0000u);
  }
  lds_st<v2u>(stat, v2u{pay[0], pay[1]});
}
#define LAMBDA [&]() NOPK __attribute__((always_inline))

#define TS(i) do {} while (0)

#define HTS(i) do {} while (0)
#define CTS 0
NOPK __global__ __launch_bounds__(512, 2) void fused_s4d_mlp(const u16* a_x, const u16* a_lar, const u16* a_aim, const u16* a_cre, const u16* a_cim,
                                                             const u16* a_ldt, const u16* a_dsk, const u16* a_w1, const u16* a_w2, u16* a_out, char* a_ws) {
  __shared__ __attribute__((aligned(16))) char smem[LDS_BYTES];
  asm volatile("" ::"v"((u32)(uintptr_t)(LAS char*)smem));
  typedef __attribute__((address_space(4))) const char* kptr;
  const kptr kas = (kptr)__builtin_amdgcn_kernarg_segment_ptr();
  const v16i ka = *(volatile __attribute__((address_space(4))) const v16i*)kas;
  const v4i kb = *(volatile __attribute__((address_space(4))) const v4i*)(kas + 64);
  const v2i kc = *(volatile __attribute__((address_space(4))) const v2i*)(kas + 80);
  SB();
#define PTR(V, I) ((char*)(((u64)(u32)(V)[2 * (I) + 1] << 32) | (u32)(V)[2 * (I)]))
#define RSRC(P, BYTES) __builtin_amdgcn_make_buffer_rsrc(P, 0, BYTES, 0x00020000)
  const int b = __builtin_amdgcn_workgroup_id_x(), xj = b & 7, r = b >> 3;
  const int tid = __builtin_amdgcn_workitem_id_x();
  const int wave = __builtin_amdgcn_readfirstlane(tid >> 6);
  const int l = tid & 63;
  const int tt = MAP2 ? 8 * (xj >> 1) + (r >> 2) : 4 * xj + (r >> 3), dt = MAP2 ? 4 * (xj & 1) + (r & 3) : r & 7;
  const v4u z4 = {0, 0, 0, 0};
#define OOFF(T) ((64 * tt + ((T) >> 3)) * 1024 + 128 * dt + 16 * ((T) & 7))
  v4u xo;

  const int w = wave & 3, wf = w & 1, wt = w >> 1;
  const int rb = 32 * xj + r, c0 = 64 * xj + 2 * r, ch = c0 + wt;
  char* const ws = PTR(kc, 0);
  u32 q_lar, q_aim, q_cre, q_cim, q_ldt, q_dsk, nonce, pfd, v_0 = 0;
  v4u xr[2];
  PIN(v_0);
  if (wave < 4) {
    v2i pc; u32 len;
    asm volatile("s_getpc_b64 %0\n.Lpf_here:\n\ts_mov_b32 %1, .Lpf_end-.Lpf_here" : "=s"(pc), "=s"(len));
    const v4i sc = {pc[0], pc[1], (int)len, 0x00020000};
    const u32 v_f = (l & 1) * 16384, s_f = (32 * w + r) * 128;
    asm volatile("buffer_load_dword %0, %1, %2, %3 offen" : "=&v"(pfd) : "v"(v_f), "s"(sc), "s"(s_f) : "memory");
    u32 v_a = ch * 256 + 4 * l, v_d = 2 * c0, v_r = (8 * rb + 2 * w) * 1024 + 16 * l;
    PIN(v_a); PIN(v_d); PIN(v_r);
    asm volatile("global_load_dwordx4 %0, %2, %3\n\tglobal_load_dwordx4 %1, %2, %3 offset:1024" : "=&v"(xr[0]), "=&v"(xr[1]) : "v"(v_r), "s"(PTR(ka, 0)) : "memory");
    asm volatile("global_load_dword %4, %8, %14\n\tglobal_load_dword %0, %7, %10\n\tglobal_load_dword %1, %7, %11\n\tglobal_load_dword %2, %7, %12\n\t"
                 "global_load_dword %3, %7, %13\n\tglobal_load_dword %5, %8, %15\n\tglobal_load_dword %6, %9, %16 sc1"
                 : "=&v"(q_lar), "=&v"(q_aim), "=&v"(q_cre), "=&v"(q_cim), "=&v"(q_ldt), "=&v"(q_dsk), "=&v"(nonce)
                 : "v"(v_a), "v"(v_d), "v"(v_0), "s"(PTR(ka, 1)), "s"(PTR(ka, 2)), "s"(PTR(ka, 3)), "s"(PTR(ka, 4)), "s"(PTR(ka, 5)),
                   "s"(PTR(ka, 6)), "s"(ws) : "memory");
  }

  auto finish = LAMBDA {
  TS(14);
  {
    int tf = tid;
    PIN(tf);
    const int tok = tf >> 3, o = tf & 7;
    const u32 a = (tok < 32 ? 98304 : 0) + (tok & 31) * 256;
    const u32 c0a = a + (((2 * o) ^ (tok & 7)) * 16), c1a = a + (((2 * o + 1) ^ (tok & 7)) * 16);
    f32x4 s0 = (lds_ld<f32x4>(c0a) + lds_ld<f32x4>(c0a + 8192)) + (lds_ld<f32x4>(c0a + 16384) + lds_ld<f32x4>(c0a + 24576));
    f32x4 s1 = (lds_ld<f32x4>(c1a) + lds_ld<f32x4>(c1a + 8192)) + (lds_ld<f32x4>(c1a + 16384) + lds_ld<f32x4>(c1a + 24576));
    const float rs = lds_ld<float>(M_RS + 4 * tok);
    const v4u R = lds_ld<v4u>(H_U + tok * 144 + 16 * o);
    v4u res;
    UNROLL for (int j = 0; j < 4; ++j) {
      const u32 xj_ = xo[j], rj = R[j];
      const float bl = j < 2 ? s0[2 * (j & 1)] : s1[2 * (j & 1)], bh = j < 2 ? s0[2 * (j & 1) + 1] : s1[2 * (j & 1) + 1];
      res[j] = pk_bf16(__builtin_fmaf(bl, rs, bf_lo(xj_) + bf_lo(rj)), __builtin_fmaf(bh, rs, bf_hi(xj_) + bf_hi(rj)));
    }
    *(GAS v4u*)((GAS char*)PTR(kb, 1) + (u32)OOFF(tf)) = res;
  }

  };

  if (wave >= 4) {
    __builtin_amdgcn_s_setprio(PRIO);
    const int mt = 4 * (xj >> 1) + (r & 3), nt = 7 * (xj & 1) + (r >> 2);
    u32 v_x[4], v_w1[4], v_l = 16 * l;
    UNROLL for (int i = 0; i < 4; ++i) {
      v_x[i] = (l >> 3) * 1024 + (((l & 7) ^ (l >> 4) ^ (4 * (i & 1))) * 16) + i * 7168;
      v_w1[i] = (l >> 4) * 3584 + (((l & 15) ^ ((l >> 4) << 2)) * 16) + i * (4 * 3584 - 1024);
      PIN(v_x[i]); PIN(v_w1[i]);
    }
    u32 s_x = (128 * mt + 32 * w) * 1024, s_w1 = (16 * w) * 3584 + 256 * nt;
    asm volatile("" : "+s"(s_x), "+s"(s_w1));
    PIN(v_l);
    SB();
    asm volatile("" ::"s"(ka), "s"(kb), "s"(kc));
    TS(25);
    const v4i sx = {ka[0], ka[1], L * D * 2, 0x00020000}, sw1 = {ka[14], ka[15], D * F * 2, 0x00020000};
    u32 nonc;
    asm volatile("global_load_dword %0, %1, %2 sc1" : "=&v"(nonc) : "v"(v_0), "s"(ws) : "memory");
    u32 so;
#define SET_M0(V) asm volatile("s_mov_b32 m0, %1" : "+s"(so) : "s"(V) : "memory")
#define GLDS(VO, SRD, I) asm volatile("buffer_load_dwordx4 %0, %1, %2 offen offset:%3 lds" ::"v"(VO), "s"(SRD), "s"(so), "n"((I) * 1024) : "memory")
    const v4i sw2 = {kb[0], kb[1], F * D * 2, 0x00020000};
    u32 v_d2[4], s_w2 = 448 * w * 1024 + 128 * dt;
#define SETG(N) do { const int G_ = (N) >> 2, T_ = G_ >> 1; \
        if (G_ >= 16) { so = s_w2 + ((N) - 64) * 8192; SET_M0((T_ & 3) * 32768 + w * 8192 + (G_ & 1) * 4096); } \
        else if (G_ & 1) { so = s_w1 + T_ * (64 * 3584); SET_M0((T_ & 3) * 32768 + 16384 + w * 4096); } \
        else { so = s_x + T_ * 128; SET_M0((T_ & 3) * 32768 + w * 4096); } } while (0)
#define GL(N) do { if ((N) >= 64) GLDS(v_d2[(N) & 3], sw2, (N) & 3); else if (((N) >> 2) & 1) GLDS(v_w1[(N) & 3], sw1, (N) & 3); else GLDS(v_x[(N) & 3], sx, (N) & 3); } while (0)
#define GLRUN(N0, N1) do { UNROLL for (int n_ = (N0); n_ < (N1); ++n_) { if ((n_ & 3) == 0) { SETG(n_); asm volatile("s_nop 0"); } GL(n_); } } while (0)
    if (__builtin_expect(r < 28, 1)) GLRUN(0, 8);
    SB();
    const v4i sws = {kc[0], kc[1], WS_END, 0x00020000};
    const __amdgpu_buffer_rsrc_t rws = RSRC(ws, WS_END);
    u32 v_w2 = (l >> 3) * 1024 + (l & 7) * 16;
    const int h = l >> 5, r32 = l & 31, blk = (l >> 4) & 1, q = (l & 15) >> 2, p = l & 3;
    u32 cown = M_CF + (v_l & 255) + 4 * w, call = M_CF + (v_l >> 2);
    PIN(v_w2); PIN(cown); PIN(call);
    asm volatile("" : "+s"(s_w2));
    const __amdgpu_buffer_rsrc_t rw2 = RSRC(PTR(kb, 0), F * D * 2);
    v4u wr[24];
#define RAW(I) wr[I] = __builtin_amdgcn_raw_buffer_load_b128(rw2, v_w2, s_w2 + (256 + 8 * (I)) * 1024, 0)
#define W2_PREP() do { UNROLL for (int i_ = 0; i_ < 4; ++i_) { v_d2[i_] = (v_w2 ^ ((v_l >> 2) & 64)) + i_ * 7168; PIN(v_d2[i_]); } } while (0)
    constexpr int ND1 = SC.d0[28] + NL, ND2 = ND1 + NB < 96 - NFILL ? ND1 + NB : (96 - NFILL > ND1 ? 96 - NFILL : ND1);
    static_assert(ND1 <= 88 && (ND1 & 3) == 0 && (ND2 & 3) == 0 && NL <= 7, "w2 schedule");
    u32 tag;
    if (__builtin_expect(r < 28, 1)) {
      u32 ab[2][4], aa[2][2];
      {
        const int row = 64 * wt + r32, z = (row >> 1) & 7;
        UNROLL for (int s = 0; s < 4; ++s) { ab[0][s] = row * 128 + (((2 * s + h) ^ z) * 16); ab[1][s] = ab[0][s] + 65536; PIN(ab[0][s]); PIN(ab[1][s]); }
        UNROLL for (int f2 = 0; f2 < 2; ++f2) {
          aa[0][f2] = 16384 + (8 * h + q) * 256 + (((8 * wf + 4 * f2 + 2 * blk + (p >> 1)) ^ (q << 2)) * 16) + 8 * (p & 1);
          aa[1][f2] = aa[0][f2] + 65536; PIN(aa[0][f2]); PIN(aa[1][f2]);
        }
      }
      u32 v_g = (v_l >> 1) & 0xf8, v_m = (v_l & 0x180) + (v_g & 0x38), ho[2];
      UNROLL for (int j = 0; j < 2; ++j) { ho[j] = WS_H + ((4 * mt + 2 * wt + j) * 112 + 8 * nt + 4 * wf) * 1024 + v_l; PIN(ho[j]); }
      PIN(v_g); PIN(v_m);
      TS(9);
      GLRUN(8, NPRO);
      SB();
      UNROLL for (int i = 0; i < NR0; ++i) { RAW(i); SB(); }
      f32x16 acc[2][2];
      UNROLL for (int i = 0; i < 2; ++i) UNROLL for (int j = 0; j < 2; ++j) UNROLL for (int e = 0; e < 16; ++e) acc[i][j][e] = 0.f;
      v4u fb[2][4][2];
      v2u fl[2][4][2], fh[2][4][2];
      v2u gm[2], gc[2];
#define RD(T, s_, j_) do { const int hi_ = ((T) >> 1) & 1, o_ = ((T) & 1) * 32768, u_ = (T) & 1; \
        if ((j_) == 0) fb[u_][s_][0] = lds_ld<v4u>(ab[hi_][s_] + o_); \
        if ((j_) == 1) fl[u_][s_][0] = lds_tr(aa[hi_][0] + o_ + (s_) * 4096); \
        if ((j_) == 2) fh[u_][s_][0] = lds_tr(aa[hi_][0] + o_ + (s_) * 4096 + 1024); \
        if ((j_) == 3) fb[u_][s_][1] = lds_ld<v4u>(ab[hi_][s_] + o_ + 4096); \
        if ((j_) == 4) fl[u_][s_][1] = lds_tr(aa[hi_][1] + o_ + (s_) * 4096); \
        if ((j_) == 5) fh[u_][s_][1] = lds_tr(aa[hi_][1] + o_ + (s_) * 4096 + 1024); } while (0)
#define RDN(T, J0, N) do { UNROLL for (int j2_ = (J0); j2_ < (J0) + (N); ++j2_) { \
        if ((T) < 7) RD(T, j2_ / 6, j2_ % 6); \
        else if (j2_ < 12) RD(T, j2_ / 3, j2_ % 3); else if (j2_ < 16) RD(T, (j2_ - 12) & 3, 3); else RD(T, ((j2_ - 16) >> 1) & 3, 4 + (j2_ & 1)); } } while (0)
#define MM(T, S, I, J) acc[I][J] = mfma32(cat(fl[(T) & 1][S][I], fh[(T) & 1][S][I]), fb[(T) & 1][S][J], acc[I][J])
#define SLOT(K, J) do { if constexpr (SC.nd[K] == 4) { GL(SC.d0[K] + 2 * (J)); GL(SC.d0[K] + 2 * (J) + 1); } \
        else if constexpr (SC.nd[K] == 2) GL(SC.d0[K] + (J)); else if constexpr (SC.nd[K] == 0) RAW(SC.r0[K] + (J)); } while (0)
#define NEXTG(K) do { if constexpr ((K) < 28) { if constexpr (SC.nd[(K) % 28] > 0 && (SC.d0[(K) % 28] & 3) == 0) SETG(SC.d0[(K) % 28]); } } while (0)
      NEXTG(0);
      TS(16);
      BAR_V(NPRO - 8 + NR0);
      TS(1);
      RDN(0, 0, 8);
      v4u ca[2], cb[2];
      u32 cf;
      SFOR(k, 0, 28)
        constexpr int t = k >> 2, s = k & 3;
        SB();
        MM(t, s, 0, 0);
        SB();
        if (s == 3) {
          BAR_V(SC.before[k] - SC.last[t + 1] - 1);
          if (CTS || t == 3 || t == 5 || t == 6) TS(17 + t);
          RDN(t + 1, 0, 2);
        }
        SLOT(k, 0);
        if (s < 3) RDN(t, 8 + 6 * s, 1);
        SB();
        MM(t, s, 0, 1);
        SB();
        if (s == 3) RDN(t + 1, 2, 3);
        if (s < 2) RDN(t, 9 + 6 * s, 2);
        if (s == 2) RDN(t, 21, 3);
        SB();
        MM(t, s, 1, 0);
        SB();
        SLOT(k, 1);
        if (s == 3) RDN(t + 1, 5, 1);
        if (s < 2) RDN(t, 11 + 6 * s, 1);
        SB();
        MM(t, s, 1, 1);
        SB();
        if (s == 3) RDN(t + 1, 6, 2);
        if (s < 2) RDN(t, 12 + 6 * s, 2);
        if constexpr (SC.nx[k]) RAW(SC.r0[k + 1] - 1);
        if (k == 10) W2_PREP();
        if (k == KGRAN) {
          const u32 so_m = WS_ST + (16 * mt + 8 * wt) * 128, so_c = WS_CS + (128 * nt + 64 * wf) * 8;
          asm volatile("buffer_load_dwordx2 %0, %8, %5, %6 offen sc1\n\tbuffer_load_dwordx2 %1, %8, %5, %6 offen offset:512 sc1\n\t"
                       "buffer_load_dwordx2 %2, %4, %5, %7 offen sc1\n\tbuffer_load_dwordx2 %3, %4, %5, %7 offen offset:256 sc1"
                       : "=&v"(gm[0]), "=&v"(gm[1]), "=&v"(gc[0]), "=&v"(gc[1]) : "v"(v_g), "s"(sws), "s"(so_m), "s"(so_c), "v"(v_m) : "memory");
        }
        if (k == 6) {
          PIN(nonc);
          tag = __builtin_amdgcn_readfirstlane(nonc) * 0x9E3779B1u + 0x7F4A7C15u;
        }
        NEXTG(k + 1);
      SEND
      v4u ho_;
#define CVT(I, J, S, E) do { const s16x2 v_ = __builtin_bit_cast(s16x2, pk_bf16(acc[I][J][8 * (S) + 2 * (E)], acc[I][J][8 * (S) + 2 * (E) + 1])); \
        const s16x2 z_ = {0, 0}; ho_[E] = __builtin_bit_cast(u32, __builtin_elementwise_max(v_, z_)); } while (0)
#define HST(I, J, S) __builtin_amdgcn_raw_buffer_store_b128(ho_, rws, ho[J] + (2 * (I) + (S)) * 1024, 0, AUX_SC1)
      UNROLL for (int m = 0; m < 25; ++m) {
        const int a = m / 5, s = m % 5, pa_ = a - 1;
        SB();
        if (a < 4) { if (s < 4) MM(7, s & 3, (a >> 1) & 1, a & 1); else acc[(a >> 1) & 1][a & 1] = mfma32(ca[(a >> 1) & 1], cb[a & 1], acc[(a >> 1) & 1][a & 1]); }
        SB();
        if (m == 0) RDN(7, 8, 4);
        if (m == 1) {
          asm volatile("s_waitcnt vmcnt(%4)" : "+v"(gm[0]), "+v"(gm[1]), "+v"(gc[0]), "+v"(gc[1]) : "n"(SC.before[28] - SC.gpos - 1 + (NL > 0 ? 1 : 0)) : "memory");
          u32 bad = ((gm[0].y ^ tag) | (gm[1].y ^ tag)) | ((gc[0].y ^ tag) | (gc[1].y ^ tag));
          PIN(bad);
          if (__builtin_expect(__builtin_amdgcn_ballot_w64(bad != 0) != 0, 0)) {
            const GAS u64* pm = (const GAS u64*)(ws + WS_ST) + 16 * (16 * mt + 8 * wt + (r32 >> 3)) + (r32 & 7);
            const GAS u64* pc = (const GAS u64*)(ws + WS_CS) + 128 * nt + 64 * wf + r32;
#pragma unroll 1
            for (int it = 0; it < SPIN_CAP; ++it) {
              const u64 m0_ = __hip_atomic_load(pm, RLX_AGENT), m1_ = __hip_atomic_load(pm + 64, RLX_AGENT);
              const u64 c0_ = __hip_atomic_load(pc, RLX_AGENT), c1_ = __hip_atomic_load(pc + 32, RLX_AGENT);
              gm[0].x = (u32)m0_; gm[1].x = (u32)m1_; gc[0].x = (u32)c0_; gc[1].x = (u32)c1_;
              if (all64((u32)(m0_ >> 32) == tag && (u32)(m1_ >> 32) == tag && (u32)(c0_ >> 32) == tag && (u32)(c1_ >> 32) == tag)) break;
            }
          }
        }
        if (m == 2) { ca[0] = v4u{h ? 0u : gc[0].x, 0u, 0u, 0u}; PIN(ca[0]); }
        if (m == 2) { cb[0] = v4u{__builtin_amdgcn_perm(gm[0].x, gm[0].x, 0x01000100u) ^ 0x80008000u, 0u, 0u, 0u}; PIN(cb[0]); }
        if (m == 3) RDN(7, 12, 4);
        if (m == 4) { cb[1] = v4u{__builtin_amdgcn_perm(gm[1].x, gm[1].x, 0x01000100u) ^ 0x80008000u, 0u, 0u, 0u}; PIN(cb[1]); }
        if (m == 5) RDN(7, 16, 4);
        if (m == 6 || m == 8) RDN(7, 14 + m, 2);
        if (m == 10) rec_post(cown, 2);
        if (m == 10) { ca[1] = v4u{h ? 0u : gc[1].x, 0u, 0u, 0u}; PIN(ca[1]); }
        if (m == 15) cf = rec_peek(call);
        if (m < NL) GLRUN(SC.d0[28] + m, SC.d0[28] + m + 1);
        if (pa_ >= 0 && s >= 1) {
          const int I = (pa_ >> 1) & 1, J = pa_ & 1, S = (s - 1) >> 1, E = 2 * ((s - 1) & 1);
          CVT(I, J, S, E); CVT(I, J, S, E + 1);
          if (E == 2) HST(I, J, S);
        }
      }
      SB();
      __builtin_amdgcn_s_setprio(0);
      TS(10);
      REC_CHECK(cf, call, 2);
      GLRUN(ND1, ND2);
      TS(3);
      WAIT_V(ND2 - ND1);
      if (l == 0) __hip_atomic_store((GAS u32*)(ws + WS_FH) + FL * ((2 * mt + wt) * 32 + 2 * nt + wf), tag, RLX_AGENT);
      TS(4);
    } else {
      __builtin_amdgcn_s_setprio(0);
      const int iu = 4 * (xj >> 1) + r - 28, ii = 14 * (xj & 1) + (iu < 14 ? iu : 0);
      u32 tch[7];
      const u32 v_t = (l >> 2) * 1024 + (l & 3) * 128;
#define TOUCH(I) tch[I] = MAP2 ? 0u : __builtin_amdgcn_raw_buffer_load_b32(rw2, v_t, (448 * (r - 28) + 64 * (I) + 16 * w) * 1024, 0)
      v4u wv[16];
      const __amdgpu_buffer_rsrc_t rw1 = RSRC(PTR(ka, 7), D * F * 2);
      UNROLL for (int i = 0; i < 16; ++i) wv[i] = __builtin_amdgcn_raw_buffer_load_b128(rw1, (l >> 3) * 3584 + (l & 7) * 16, (128 * w + 8 * i) * 3584 + 128 * ii, 0);
      SB();
      TOUCH(4); TOUCH(5);
      SB();
      WAIT_V(16);
      NBAR();
      float cs[8];
      u32 one_lo = 0x00003f80u, one_hi = 0x3f800000u;
      PIN(one_lo); PIN(one_hi);
      UNROLL for (int j = 0; j < 8; ++j) cs[j] = 0.f;
      UNROLL for (int i = 0; i < 16; ++i) UNROLL for (int j = 0; j < 4; ++j) { const u32 u = wv[i][j]; cs[2 * j] = dot2(u, one_lo, cs[2 * j]); cs[2 * j + 1] = dot2(u, one_hi, cs[2 * j + 1]); }
      lds_st<f32x4>(98304 + (8 * w + (l >> 3)) * 272 + 32 * (l & 7), f32x4{cs[0], cs[1], cs[2], cs[3]});
      lds_st<f32x4>(98304 + (8 * w + (l >> 3)) * 272 + 32 * (l & 7) + 16, f32x4{cs[4], cs[5], cs[6], cs[7]});
      rec_post(cown, 1);
      UNROLL for (int i = 0; i < 8; ++i) RAW(i);
      SB();
      rec_wait<1>(call, 1);
      PIN(nonc);
      tag = __builtin_amdgcn_readfirstlane(nonc) * 0x9E3779B1u + 0x7F4A7C15u;
      {
        float s = 0.f;
        UNROLL for (int i = 0; i < 8; ++i) s += lds_ld<float>(98304 + (8 * (l >> 4) + i) * 272 + 4 * (16 * w + (l & 15)));
        rec_post(cown, 2);
        const auto s16 = __builtin_amdgcn_permlane16_swap(asu(s), asu(s), false, false);
        s = asf(s16[0]) + asf(s16[1]);
        const auto s32 = __builtin_amdgcn_permlane32_swap(asu(s), asu(s), false, false);
        s = asf(s32[0]) + asf(s32[1]);
        const float hi = rbf(s);
        if (iu < 14 && l < 16) __hip_atomic_store((GAS u64*)(ws + WS_CS) + 64 * ii + 16 * w + l, ((u64)tag << 32) | pk_bf16(hi, s - hi), RLX_AGENT);
      }
      TS(2);
      UNROLL for (int i = 8; i < 16; ++i) RAW(i);
      TOUCH(6);
      UNROLL for (int i = 16; i < SC.r0[28]; ++i) RAW(i);
      UNROLL for (int i = 0; i < 4; ++i) { TOUCH(i); SB(); }
      static_assert(SC.r0[28] >= 16, "w2 schedule");
      UNROLL for (int i = 0; i < 7; ++i) asm volatile("" ::"v"(tch[i]));
      __builtin_amdgcn_s_waitcnt(0x0F70);
      W2_PREP();
      rec_wait<1>(call, 2);
      GLRUN(64, ND2);
      SB();
    }
    int l2 = tid & 63;
    PIN(l2);
    const int h2 = l2 >> 5, q2 = (l2 & 15) >> 2, p2 = l2 & 3, r2 = l2 & 31;
    u32 a2[2][2];
    UNROLL for (int d2 = 0; d2 < 2; ++d2) {
      a2[0][d2] = w * 8192 + (4 * h2 + q2) * 128 + (((4 * d2 + 2 * ((l2 >> 4) & 1) + (p2 >> 1)) ^ (((q2 >> 1) & 1) << 2)) * 16) + 8 * (p2 & 1);
      a2[1][d2] = a2[0][d2] + 65536; PIN(a2[0][d2]); PIN(a2[1][d2]);
    }
#define W2_FRAG(K, D2) cat(lds_tr(a2[((K) >> 3) & 1][D2] + (((K) >> 2) & 1) * 32768 + ((K) & 3) * 2048), lds_tr(a2[((K) >> 3) & 1][D2] + (((K) >> 2) & 1) * 32768 + ((K) & 3) * 2048 + 1024))
    u32 wa[2], v_f = 4 * FL * (l2 < 7 ? l2 : 0), fh0;
    wa[0] = w * 8192 + (l2 >> 3) * 128 + (((l2 & 7) ^ (((l2 >> 4) & 1) << 2)) * 16); wa[1] = wa[0] + 65536;
#define W2_ST(I) lds_st<v4u>(wa[((I) >> 4) & 1] + (((I) >> 3) & 1) * 32768 + ((I) & 7) * 1024, wr[I])
    u32 s_h = WS_H + ((2 * tt) * 112 + 28 * w) * 1024, s_f = WS_FH + (tt * 32 + 7 * w) * 4 * FL;
    u32 v_h[2] = {(u32)(16 * l2), (u32)(16 * l2 + 112 * 1024)};
    asm volatile("" : "+s"(s_h), "+s"(s_f));
    PIN(v_h[0]); PIN(v_h[1]); PIN(wa[0]); PIN(wa[1]); PIN(v_f);
    constexpr int ND3 = 96 - NFILL > ND2 ? 96 - NFILL : ND2;
    GLRUN(ND2, ND3);
    __builtin_amdgcn_s_sleep(PDLY);
    TS(5);
    asm volatile("buffer_load_dword %0, %1, %2, %3 offen sc1" : "=&v"(fh0) : "v"(v_f), "s"(sws), "s"(s_f) : "memory");
    GLRUN(ND3, 96);
    SB();
    UNROLL for (int i = SC.r0[28]; i < 24; ++i) { RAW(i); SB(); }
    asm volatile("s_waitcnt vmcnt(%1)" : "+v"(fh0) : "n"(96 - ND3 + 24 - SC.r0[28]) : "memory");
    TS(11);
    if (!all64(fh0 == tag)) poll_flag((const GAS u32*)(ws + s_f + v_f), tag);
    AFTER_POLL();
    __builtin_amdgcn_s_setprio(PRIO);
    TS(6);
    f32x16 acc[2][2];
    UNROLL for (int i = 0; i < 2; ++i) UNROLL for (int j = 0; j < 2; ++j) UNROLL for (int e = 0; e < 16; ++e) acc[i][j][e] = 0.f;
    v4u hb[28][2];
#define H_LOAD(K, T2) hb[K][T2] = __builtin_amdgcn_raw_buffer_load_b128(rws, v_h[T2] + ((K) & 3) * 1024, s_h + ((K) >> 2) * 4096, AUX_SC1)
#define H_N(N) do { if ((N) < 40) H_LOAD((N) < 40 ? (N) >> 1 : 0, (N) & 1); else if ((N) < 56) H_LOAD(20 + ((N) & 7), ((N) >> 3) & 1 ? 0 : 1); } while (0)
    UNROLL for (int n = 0; n < 2 * HWIN; ++n) { H_N(n); SB(); }
    v4u fa[28][2];
    UNROLL for (int k = 0; k < 2; ++k) { fa[k][0] = W2_FRAG(k, 0); fa[k][1] = W2_FRAG(k, 1); }
    SB();
    u32 pw[2][2];
    SFOR(k, 0, 20)
      constexpr int kn = k + 2, st = 2 * (k - 2);
      constexpr int n0 = 2 * HWIN + 2 * k + (k > HGROW ? 2 * MIN_(k - HGROW, HADD) : 0), nl = (k >= HGROW && k < HGROW + HADD) ? 2 : 1;
      acc[0][0] = mfma32(fa[k][0], hb[k][0], acc[0][0]);
      SB();
      fa[kn][0] = W2_FRAG(kn, 0);
      SB();
      acc[1][0] = mfma32(fa[k][1], hb[k][0], acc[1][0]);
      SB();
      H_N(n0); if (nl == 2) { SB(); H_N(n0 + 1); }
      if (k >= 2 && k < 14) W2_ST(k >= 2 && k < 14 ? st : 0);
      SB();
      acc[0][1] = mfma32(fa[k][0], hb[k][1], acc[0][1]);
      SB();
      fa[kn][1] = W2_FRAG(kn, 1);
      SB();
      acc[1][1] = mfma32(fa[k][1], hb[k][1], acc[1][1]);
      SB();
      H_N(n0 + nl); if (nl == 2) { SB(); H_N(n0 + 3); }
      if (k >= 2 && k < 14) W2_ST(k >= 2 && k < 14 ? st + 1 : 0);
      if (k == 0) TS(26);
      if (k == 14) xo = __builtin_amdgcn_raw_buffer_load_b128(RSRC(PTR(ka, 0), L * D * 2), OOFF(256 + 64 * w + l2), 0, 0);
      if (k == KP1) rec_post(cown, 3);
      if (k == KP2) rec_post(cown, 4);
      if (k == 16) { UNROLL for (int i = 0; i < 2; ++i) UNROLL for (int e = 0; e < 2; ++e) { pw[i][e] = w * 8192 + r2 * 256 + (((4 * i + 2 * e + h2) ^ (r2 & 7)) * 16); PIN(pw[i][e]); } }
      SB();
    SEND
    TS(7);
#define P_ST(I, J, E) lds_st<f32x4>(pw[(E) >> 1][(E) & 1] + ((J) ? 0 : 98304) + (I) * 128, f32x4{acc[I][J][4 * (E)], acc[I][J][4 * (E) + 1], acc[I][J][4 * (E) + 2], acc[I][J][4 * (E) + 3]})
    SFOR(m, 0, 32)
      constexpr int j = m >> 4, mm = m & 15;
      constexpr int kk = (j == 0 || mm < 8) ? 20 + (mm >> 1) : 24 + (mm & 3), i = (j == 0 || mm < 8) ? (mm & 1) : ((mm >> 2) & 1);
      constexpr int n0 = 2 * HWIN + 40 + 2 * HADD;
      acc[i][j] = mfma32(fa[kk][i], hb[kk][j], acc[i][j]);
      SB();
      if constexpr (m < 12) fa[22 + (m >> 1)][m & 1] = W2_FRAG(22 + (m >> 1), m & 1);
      if (m & 1) H_N(n0 + (m >> 1));
      if (m >= 17 && m < 25) P_ST(((m - 17) >> 2) & 1, 0, (m - 17) & 3);
      if (m == 25) rec_post(cown, 5);
      if (m >= 29) P_ST(0, 1, (m - 29) & 3);
      SB();
    SEND
    P_ST(0, 1, 3);
    UNROLL for (int e = 0; e < 4; ++e) P_ST(1, 1, e);
    rec_post(cown, 6);
    TS(8);
    rec_spin(call + (M_HF - M_CF), 8);
    rec_spin(call, 6);
    finish();
    asm volatile("" : "=v"(q_lar), "=v"(q_aim), "=v"(q_cre), "=v"(q_cim), "=v"(q_ldt), "=v"(q_dsk), "=v"(nonce), "=v"(pfd), "=v"(xr[0]), "=v"(xr[1]));
  }
  int role = wave;
  asm volatile("" : "+s"(role));
  if (role < 4) {
    const int hw = wave, chl = wt, hm = wf;
    int l = tid & 63;
    PIN(l);
    const int g = l >> 4, i16 = l & 15;
    u32 hown = M_HF + 16 * i16 + 4 * hw, hall = M_HF + 4 * l;
    PIN(hown); PIN(hall);
    const u32 hmate = hown ^ 4;
    const __amdgpu_buffer_rsrc_t rws = RSRC(ws, WS_END);
    lds_st<u32>(M_CF + 256 * hw + 4 * l, hw < 2 ? 0u : hw == 2 ? 0x3c00u : 0x4c00u);
    const u32 img = H_U + hw * 6256, tb = H_T + chl * 2640 + 8, eb = H_S + chl * 8320;
    u32 fa_[4], fb_[4], xa[17], fa4[4];
    {
      const u32 o = img + 16 * g, one = M_ONE + 16 * g, big = one + 256;
      const int e0 = i16 & 7, e1 = i16 < 8 ? 2 * (i16 & 3) : 4 * (i16 & 1), p1 = i16 < 8 ? 2 + (i16 >> 2) : i16 >> 1, p2 = i16 < 8 ? i16 : 8;
      fa_[0] = o + (16 + (i16 & 3)) * 272; fb_[0] = i16 < 4 ? one : o + (19 + (i16 >> 2)) * 272;
      fa_[1] = e0 ? o + (e0 - 1) * 272 : big; fb_[1] = i16 < 8 ? one : o + 7 * 272;
      fa_[2] = e1 ? o + (e1 - 1) * 272 : big; fb_[2] = o + (6 + p1) * 272;
      fa_[3] = o + 15 * 272; fb_[3] = p2 ? o + (6 + p2) * 272 : one;
      UNROLL for (int i = 0; i < 4; ++i) { PIN(fa_[i]); PIN(fb_[i]); }
    }
    u32 lnr = H_U + (i16 >> 2) * 1088 + (128 * hw + 16 * g + 4 * (l & 3)) * 2, xno = XNL == 0 ? WS_XN + rb * 8192 + (128 * hw + l) * 16 : XNL == 1 ? WS_XN + (rb >> 2) * 32768 + (rb & 3) * 32 + (64 * hw + (l >> 1)) * 128 + (l & 1) * 16
                                                                                                       : WS_XN + (rb >> 1) * 16384 + (rb & 1) * 32 + (64 * hw + (l >> 1)) * 64 + (l & 1) * 16;
    u32 lnw = H_U + 2 * hw * 1088 + 16 * l, sto = ((l >> 3) & 1) * 128 + (l & 7) * 8;
    PIN(lnr); PIN(xno); PIN(lnw); PIN(sto);
#define XA_SET() do { const int T = 127 - 64 * hm - l, sh = 28 - __builtin_clz(T | 8); \
      u32 pat = (u32)(0x040812123333ffffull >> (16 * sh)) | (T == 16 ? 0x8000u : 0u); \
      PIN(pat); \
      xa[0] = tb + 4 * (((16 * T + 15 + (1 << sh >> 1)) >> sh) + 128 * sh); \
      UNROLL for (int e = 0; e < 16; e += 4) \
        asm("v_bfe_i32 %0, %4, %5, 1\n\tv_bfe_i32 %1, %4, %5+1, 1\n\tv_bfe_i32 %2, %4, %5+2, 1\n\tv_bfe_i32 %3, %4, %5+3, 1\n\t" \
            "v_lshl_add_u32 %0, %0, 2, %6\n\tv_lshl_add_u32 %1, %1, 2, %0\n\tv_lshl_add_u32 %2, %2, 2, %1\n\tv_lshl_add_u32 %3, %3, 2, %2" \
            : "=&v"(xa[e + 1]), "=&v"(xa[e + 2]), "=&v"(xa[e + 3]), "=&v"(xa[e + 4]) : "v"(pat), "n"(e), "v"(xa[e])); \
      UNROLL for (int e = 0; e < 17; ++e) PIN(xa[e]); } while (0)
    if (XAPOS == 1) XA_SET();
#define STAGE(I) do { if ((I) == 0) { HTS(16); HBAR(); } else if ((BARS >> (I)) & 1) { HTS(16 + __builtin_popcount(BARS & ((2u << (I)) - 1))); if (r < 28) NBAR(); } } while (0)
    static_assert(__builtin_popcount(BARS) == 7 && !(BARS & 1), "seven barriers");
    __builtin_amdgcn_s_setprio(LNPRIO);
    TS(10);
    asm volatile("s_waitcnt vmcnt(7)" : "+v"(xr[0]), "+v"(xr[1]), "+v"(pfd)::"memory");
    TS(12);
    u32 tag;
    ln_rows(xr, lnw, M_ST + 8 * hw, LAMBDA {
      asm volatile("s_waitcnt vmcnt(0)" : "+v"(q_lar), "+v"(q_aim), "+v"(q_cre), "+v"(q_cim), "+v"(q_ldt), "+v"(q_dsk), "+v"(nonce)::"memory");
      tag = __builtin_amdgcn_readfirstlane(nonce) * 0x9E3779B1u + 0x7F4A7C15u;
      return (tag >> (3 * hw)) & 0x00070007u; });
    TS(13);
    STAGE(0);
    {
      const u32 stv = lds_ld<u32>(M_ST + (sto & 56) / 2);
      v4u o[2];
      UNROLL for (int k = 0; k < 2; ++k) o[k] = cat(lds_tr(lnr + 128 * k), lds_tr(lnr + 128 * k + 4 * 1088));
      rec_post(hown, 2);
      UNROLL for (int k = 0; k < 2; ++k) __builtin_amdgcn_raw_buffer_store_b128(o[k], rws, xno + (XNL == 0 ? 1024 : XNL == 1 ? 4096 : 2048) * k, 0, AUX_SC1);
      if (tid < 8) __builtin_amdgcn_raw_buffer_store_b64(v2u{stv, tag}, rws, sto, WS_ST + rb * 128, AUX_SC1);
      __builtin_amdgcn_s_setprio(0);
      HTS(1);
    }
    STAGE(1);
    STAGE(2);
    const u32 psel = hm ? 0x03020c0cu : 0x01000c0cu, csel = chl ? 0x03020c0cu : 0x01000c0cu;
    const float p_lar = asf(__builtin_amdgcn_perm(q_lar, q_lar, psel)), p_aim = asf(__builtin_amdgcn_perm(q_aim, q_aim, psel));
    const float p_ldt = asf(__builtin_amdgcn_perm(q_ldt, q_ldt, csel));
    const float dtv = rbf(exp_f(p_ldt));
    const float beta = p_aim * dtv;
    float a2 = rbf(__builtin_amdgcn_exp2f(p_lar * 1.44269504088896341f)) * dtv * -1.44269504088896341f;
    float th = beta * 0.15915494309189535f;
    float tl = __builtin_fmaf(beta, 6.42063831672e-09f, __builtin_fmaf(beta, 0.15915494309189535f, -th));
    PIN(a2); PIN(th); PIN(tl);
    STAGE(3);
    u32 fr[24];
#define KEEP(I, Z) do { const cpx z_ = (Z); fr[I] = pk_f16(z_.r, z_.i); PIN(fr[I]); } while (0)
    cpx z128;
    {
      const cpx z1 = zpow(a2, th, tl, 1.f);
      cpx e; e.r = z1.r * 16.f; e.i = z1.i * 16.f;
      UNROLL for (int i = 0; i < 7; ++i) { KEEP(i, e); if (i < 6) e = cmul(e, z1); }
      z128 = zpow(a2, th, tl, 128.f);
      PIN(z128.r); PIN(z128.i);
    }
    STAGE(4);
    cpx z8c;
    {
      cpx e = z128;
      UNROLL for (int i = 7; i < 14; ++i) { KEEP(i, e); if (i < 13) e = cmul(e, z128); }
      const cpx z1024 = zpow(a2, th, tl, 1024.f);
      e.r = z1024.r * 16.f; e.i = z1024.i * 16.f;
      KEEP(14, z1024); KEEP(15, e);
      z8c = zpow(a2, -th, -tl, 8.f);
      PIN(z8c.r); PIN(z8c.i);
    }
    STAGE(5);
    const u32 ma = q_cre & 0x7f807f80u, mb = q_cim & 0x7f807f80u;
    u32 mx = umax(umax(ma << 16, mb << 16), umax(ma, mb));
    mx = dpp_max<0xB1, 0xf>(mx); mx = dpp_max<0x4E, 0xf>(mx); mx = dpp_max<0x124, 0xf>(mx); mx = dpp_max<0x128, 0xf>(mx);
    mx = dpp_max<0x142, 0xa>(mx); mx = dpp_max<0x143, 0xc>(mx);
    int ex = (int)(__builtin_amdgcn_readlane(mx, 63) >> 23) + 1;
    ex = ex < 32 ? 32 : (ex > 230 ? 230 : ex);
    const float cscale = asf((u32)(254 + 4 - ex) << 23);
    const float yscale = asf((u32)(ex - 17) << 23) * dtv;
    {
      const cpx z32c = zpow(a2, -th, -tl, 32.f), z64c = cmul(z32c, z32c);
      KEEP(20, z32c); KEEP(21, z64c); KEEP(22, cmul(z64c, z32c));
      cpx y;
      y.r = asf(__builtin_amdgcn_perm(q_cre, q_cre, psel)) * cscale; y.i = asf(__builtin_amdgcn_perm(q_cim, q_cim, psel)) * -cscale;
      UNROLL for (int i = 0; i < 4; ++i) { KEEP(16 + i, y); if (i < 3) y = cmul(y, z8c); }
    }
    STAGE(6);
    u32 uoff = XNL == 0 ? (l >> 1) * 8192 + (l & 1) * 16 : XNL == 1 ? (l >> 3) * 32768 + (l & 7) * 16 : (l >> 2) * 16384 + (l & 3) * 16, xoo = (l >> 3) * 1024 + (l & 7) * 16;
    const u32 s_u = WS_XN + 64 * hw * 8192 + c0 * (XNL == 0 ? 16 : XNL == 1 ? 64 : 32), s_st = WS_ST + (8 * tt + 2 * hw) * 128;
    {
      u32 sw[6];
      UNROLL for (int i = 0; i < 6; ++i) { sw[i] = img + 4 * l + 1088 * i; PIN(sw[i]); }
      REC_WAIT(hall, 2);
      __builtin_amdgcn_s_setprio(3);
      SB();
      UNROLL for (int i = 0; i < 23; ++i) lds_st<u32>(sw[i >> 2] + 272 * (i & 3), fr[i]);
      SB();
      __builtin_amdgcn_s_setprio(0);
    }
    v4u op[4][4], oa[2][4], ob[2][4];
#define OP_LD(T) do { UNROLL for (int s_ = 0; s_ < 4; ++s_) { oa[(T) & 1][s_] = lds_ld<v4u>(fa_[T] + 64 * s_); ob[(T) & 1][s_] = lds_ld<v4u>(fb_[T] + 64 * s_); } SB(); } while (0)
#define OP_MUL(T) do { UNROLL for (int s_ = 0; s_ < 4; ++s_) op[T][s_] = cmulh(oa[(T) & 1][s_], ob[(T) & 1][s_]); \
      asm volatile("s_nop 1" : "+v"(op[T][0]), "+v"(op[T][1]), "+v"(op[T][2]), "+v"(op[T][3])); SB(); } while (0)
#define OP_MM(T) do { __builtin_amdgcn_s_setprio(3); SB(); kacc[T] = f32x4{0.f, 0.f, 0.f, 0.f}; \
      UNROLL for (int s_ = 0; s_ < 4; ++s_) kacc[T] = mfma16h(op[(T) + 1][s_], op[0][s_], kacc[T]); SB(); __builtin_amdgcn_s_setprio(0); PIN(kacc[T]); SB(); } while (0)
#define OP_CVT(T) do { const f32x4 v_ = kacc[T]; \
      kp[T][0] = pk_f16(v_[0], v_[1]); kp[T][1] = pk_f16(v_[2], v_[3]); PIN(kp[T][0]); PIN(kp[T][1]); } while (0)
    f32x4 kacc[3];
    u32 kp[3][2];
    PIN(uoff); PIN(xoo);
    OP_LD(0);
    if (XAPOS == 2) XA_SET();
    STAGE(7);
    v4u uv[2];
    v2u st0;
#define GATHER() do { __builtin_amdgcn_s_setprio(3); \
      UNROLL for (int i = 0; i < 2; ++i) uv[i] = __builtin_amdgcn_raw_buffer_load_b128(rws, uoff, s_u + i * 32 * 8192, AUX_SC1); \
      st0 = __builtin_amdgcn_raw_buffer_load_b64(rws, sto, s_st, AUX_SC1); \
      xo = __builtin_amdgcn_raw_buffer_load_b128(RSRC(PTR(ka, 0), L * D * 2), xoo, (64 * tt + 8 * hw) * 1024 + 128 * dt, 0); \
      __builtin_amdgcn_s_setprio(0); SB(); } while (0)
    if (GPOSN == 0) GATHER();
    OP_LD(1); OP_MUL(0);
    STAGE(8);
    if (GPOSN == 1) GATHER();
    OP_LD(2); OP_MUL(1); OP_MM(0);
    STAGE(9);
    if (GPOSN == 2) GATHER();
    OP_LD(3); OP_MUL(2); OP_MM(1);
    STAGE(10);
    OP_MUL(3); OP_MM(2); OP_CVT(0); OP_CVT(1);
    u32 ta0 = tb + 2 * hm + 4 * (128 * (g >> 1) + 4 * (g & 1) + 8 * i16), ta1 = tb + 2 * hm + 4 * (g < 2 ? 256 + 64 * g + 4 * i16 : 384 + 64 * (g - 2) + 2 * i16);
    u32 ta1b = ta1 + (g < 2 ? 8 : 128), ta2 = tb + 2 * hm + (g < 3 ? 4 * (512 + 64 * g + i16) : -4);
    OP_CVT(2);
    STAGE(11);
    TS(24);
    __builtin_amdgcn_s_setprio(HPRIO);
    lds_st<u16>(ta0, (u16)kp[0][0]); lds_st<u16>(ta0 + 4, (u16)(kp[0][0] >> 16)); lds_st<u16>(ta0 + 8, (u16)kp[0][1]); lds_st<u16>(ta0 + 12, (u16)(kp[0][1] >> 16));
    lds_st<u16>(ta1, (u16)kp[1][0]); lds_st<u16>(ta1 + 4, (u16)(kp[1][0] >> 16)); lds_st<u16>(ta1b, (u16)kp[1][1]); lds_st<u16>(ta1b + 4, (u16)(kp[1][1] >> 16));
    lds_st<u16>(ta2, (u16)(g < 3 ? kp[2][0] : 0u));
    if (g < 2) { lds_st<u16>(ta2 + 64, (u16)(kp[2][0] >> 16)); lds_st<u16>(ta2 + 128, (u16)kp[2][1]); lds_st<u16>(ta2 + 192, (u16)(kp[2][1] >> 16)); }
    rec_post(hown, 3);
    if (XAPOS == 0) XA_SET();
    u32 uw = H_U + (l & 1) * 4608 + 512 + (64 * hw + (l >> 1)) * 16;
    u32 ew = eb + 32 * (64 * hm + l), zw = 16 * l + (l < 16 ? H_U + chl * 4608 + 256 * hm : eb + 4096 + hm * 4160 - 256);
    PIN(uw); PIN(ew); PIN(zw);
    STAGE(12);
    rec_spin(hall, 3);
    TS(3);
    {
      u32 kv[17];
      UNROLL for (int e = 0; e < 17; ++e) kv[e] = lds_ld<u32>(xa[e]);
      SB();
      STAGE(13);
      u32 tgb[4];
      UNROLL for (int j = 0; j < 4; ++j) tgb[j] = (tag >> (3 * j)) & 0x00070007u;
      UNROLL for (int i = 0; i < 2; ++i) UNROLL for (int j = 0; j < 4; ++j) uv[i][j] ^= tgb[j];
      if (__builtin_expect(!all64(((((uv[0].x | uv[0].y) | (uv[0].z | uv[0].w)) | ((uv[1].x | uv[1].y) | (uv[1].z | uv[1].w))) & 0x00070007u) == 0), 0)) {
        UNROLL for (int i = 0; i < 2; ++i) {
#pragma unroll 1
          for (int it = 0; it < SPIN_CAP; ++it) {
            if ((((uv[i].x | uv[i].y) | (uv[i].z | uv[i].w)) & 0x00070007u) == 0) break;
            FENCE();
            uv[i] = __builtin_amdgcn_raw_buffer_load_b128(rws, uoff, s_u + i * 32 * 8192, AUX_SC1);
            UNROLL for (int j = 0; j < 4; ++j) uv[i][j] ^= tgb[j];
            __builtin_amdgcn_s_sleep(PSLP);
          }
        }
      }
      lds_st<v4u>(uw, uv[0]); lds_st<v4u>(uw + 512, uv[1]);
      if (l < 18) lds_st<v4u>(zw, z4);
      SB();
      u32 ev[9];
#define LO_ " dst_sel:WORD_0 dst_unused:UNUSED_PAD src0_sel:WORD_0 src1_sel:WORD_1\n\t"
#define HI_ " dst_sel:WORD_1 dst_unused:UNUSED_PRESERVE src0_sel:WORD_0 src1_sel:WORD_1\n\t"
      asm("v_add_f16_sdwa %0, %4, %4" LO_ "v_add_f16_sdwa %1, %6, %6" LO_ "v_add_f16_sdwa %2, %8, %8" LO_ "v_add_f16_sdwa %3, %10, %10" LO_
          "v_add_f16_sdwa %0, %5, %5" HI_ "v_add_f16_sdwa %1, %7, %7" HI_ "v_add_f16_sdwa %2, %9, %9" HI_ "v_add_f16_sdwa %3, %11, %11" HI_ "s_nop 0"
          : "=&v"(ev[0]), "=&v"(ev[1]), "=&v"(ev[2]), "=&v"(ev[3])
          : "v"(kv[0]), "v"(kv[1]), "v"(kv[2]), "v"(kv[3]), "v"(kv[4]), "v"(kv[5]), "v"(kv[6]), "v"(kv[7]));
      asm("v_add_f16_sdwa %0, %5, %5" LO_ "v_add_f16_sdwa %1, %7, %7" LO_ "v_add_f16_sdwa %2, %9, %9" LO_ "v_add_f16_sdwa %3, %11, %11" LO_
          "v_add_f16_sdwa %0, %6, %6" HI_ "v_add_f16_sdwa %1, %8, %8" HI_ "v_add_f16_sdwa %2, %10, %10" HI_ "v_add_f16_sdwa %3, %12, %12" HI_
          "v_add_f16_sdwa %4, %13, %13" LO_ "s_nop 0"
          : "=&v"(ev[4]), "=&v"(ev[5]), "=&v"(ev[6]), "=&v"(ev[7]), "=&v"(ev[8])
          : "v"(kv[8]), "v"(kv[9]), "v"(kv[10]), "v"(kv[11]), "v"(kv[12]), "v"(kv[13]), "v"(kv[14]), "v"(kv[15]), "v"(kv[16]));
      lds_st<v4u>(ew, v4u{ev[0], ev[1], ev[2], ev[3]}); lds_st<v4u>(ew + 16, v4u{ev[4], ev[5], ev[6], ev[7]});
      v4u eb0, eb1;
      UNROLL for (int e = 0; e < 4; ++e) { eb0[e] = __builtin_amdgcn_alignbit(ev[e + 1], ev[e], 16); eb1[e] = __builtin_amdgcn_alignbit(ev[e + 5], ev[e + 4], 16); }
      lds_st<v4u>(ew + 4160, eb0); lds_st<v4u>(ew + 4176, eb1);
    }
    rec_post(hown, 4);
    {
      const int x0 = 2047 - 16 * (1 + 8 * hm) - i16 + 8 * g;
      const u32 a = eb + ((i16 & 1) ? 0 : 4160) + 4 * (x0 >> 1) - 512 * 7 - 192;
      UNROLL for (int i = 0; i < 4; ++i) { fa4[i] = a + 1024 * (3 - i); PIN(fa4[i]); }
    }
#define FA_H(W, MI, H) (*(const LAS v2u_a4*)(uintptr_t)(fa4[(W) >> 1] + 512 * (1 - ((W) & 1)) + 192 - 64 * (MI) + 8 * (H)))
    v2u FA[4][8][2];
    v4u G[4][8];
    f32x4 acc[8], ud[4];
    const u32 ub = H_U + chl * 4608 + 512;
    u32 ga = ub + 32 * i16 + 16 * g - 32 * (1 + 8 * hm) - 192, ua = ub + 2048 * hm + 32 * i16 + 8 * g;
    float dsv = asf(__builtin_amdgcn_perm(q_dsk, q_dsk, csel)) * (1.0f / 1024.0f);
    u32 xw, xrd, yo;
    PIN(ga); PIN(ua); PIN(dsv);
    TS(4);
    rec_spin(hall, 4);
    __builtin_amdgcn_s_setprio(HPRIO2);
    TS(5);
    {
      v2u uu[4];
      u32 pf;
#define CV_RD(MI, N) do { if constexpr ((N) < 2) FA[MI][0][(N) & 1] = FA_H(0, MI, (N) & 1); \
        else if constexpr ((N) < 10) G[MI][((N) - 2) & 7] = lds_ld<v4u>(ga + 512 * ((N) - 2) + 192 - 64 * (MI)); \
        else FA[MI][((N) - 8) >> 1][(N) & 1] = FA_H(((N) - 8) >> 1, MI, (N) & 1); SB(); } while (0)
      SFOR(n, 0, 12) CV_RD(0, n); SEND
      SFOR(i, 0, 108)
        constexpr int mi = i / 36, j = i % 36;
        constexpr int w = j < 8 ? 0 : j < 15 ? 1 : j < 21 ? 2 : j < 26 ? 3 : j < 30 ? 4 : j < 33 ? 5 : j < 35 ? 6 : 7;
        constexpr int v = j - (8 * w - w * (w - 1) / 2);
        acc[v + w] = mfma16h(cat(FA[mi][w][0], FA[mi][w][1]), G[mi][v], i < 8 ? f32x4{0.f, 0.f, 0.f, 0.f} : acc[v + w]);
        SB();
        if (i == 35) TS(25);
        if (i == 71) TS(26);
        if constexpr (mi == 0) { if constexpr (j < 12) CV_RD(0, 12 + j); else CV_RD(1, j - 12); }
        else if constexpr (j < 24) CV_RD(mi + 1, j);
        else if constexpr (mi == 1 && j < 28) { uu[j - 24] = lds_ld<v2u>(ua + 512 * (j - 24)); SB(); }
        else if constexpr (mi == 1 && j == 28) { xw = H_S + hw * 4160 + 16 * l; PIN(xw); SB(); }
        else if constexpr (mi == 1 && j == 29) { xrd = H_S + (hw ^ 1) * 4160 + 16 * l; PIN(xrd); SB(); }
        else if constexpr (mi == 1 && j == 30) { yo = WS_Y + ch * 4096 + hm * 2048 + 16 * l; PIN(yo); SB(); }
        else if constexpr (mi == 2 && j == 24) rec_post(hown, 5);
        else {
          constexpr int e = mi == 1 ? j - 31 : j - 20;
          const u32 up = (e & 2) ? uu[e >> 2].y : uu[e >> 2].x;
          const f16x2 uh = __builtin_bit_cast(f16x2, up);
          ud[e >> 2][e & 3] = (float)((e & 1) ? uh.y : uh.x) * dsv; PIN(ud[e >> 2][e & 3]); SB();
        }
      SEND
      TS(27);
      pf = rec_peek(hmate);
#define TILE(N, P) do { UNROLL for (int w_ = 0; w_ <= (N); ++w_) { acc[N] = mfma16h(cat(FA[3][w_][0], FA[3][w_][1]), G[3][(N) - w_], acc[N]); SB(); \
        if ((P) >= 0 && w_ >= 2 && w_ <= 5) { ud[(P) & 3][w_ - 2] = __builtin_fmaf(acc[(P) & 7][w_ - 2], yscale, ud[(P) & 3][w_ - 2]); PIN(ud[(P) & 3][w_ - 2]); SB(); } } \
        asm volatile("" ::"v"(acc[N])); SB(); } while (0)
#define SEND_(N) do { lds_st<f32x4>(xw + 1024 * ((N) & 3), acc[N]); SB(); } while (0)
#define OWN(N) do { UNROLL for (int e_ = 0; e_ < 4; ++e_) ud[(N) & 3][e_] = __builtin_fmaf(acc[N][e_], yscale, ud[(N) & 3][e_]); PIN(ud[(N) & 3]); SB(); } while (0)
      SB();
      if (hm) {
        TILE(3, -1); TILE(2, -1); TILE(1, -1); TILE(0, -1); REC_CHECK(pf, hmate, 5); SEND_(3); TILE(4, -1); SEND_(2); SEND_(1); TILE(5, -1); SEND_(0); rec_post(hown, 6); TS(28);
        TILE(6, 4); TILE(7, 5); OWN(6); OWN(7);
      } else {
        TILE(4, -1); TILE(7, -1); REC_CHECK(pf, hmate, 5); SEND_(4); TILE(6, -1); SEND_(7); TILE(5, -1); TILE(1, -1); SEND_(6); TILE(3, -1); TILE(0, -1); SEND_(5); rec_post(hown, 6); TS(28);
        TILE(2, -1); OWN(1); OWN(3); OWN(0); OWN(2);
      }
    }
    TS(6);
    {
      rec_spin(hmate, 6);
      u32 o[4][2];
      UNROLL for (int i = 0; i < 4; ++i) {
        const f32x4 y = lds_ld<f32x4>(xrd + 1024 * i);
        o[i][0] = pk_bf16(__builtin_fmaf(y[0], yscale, ud[i][0]), __builtin_fmaf(y[1], yscale, ud[i][1]));
        o[i][1] = pk_bf16(__builtin_fmaf(y[2], yscale, ud[i][2]), __builtin_fmaf(y[3], yscale, ud[i][3]));
      }
      UNROLL for (int i = 0; i < 4; i += 2) {
        const auto s0 = __builtin_amdgcn_permlane16_swap(o[i][0], o[i + 1][0], false, false);
        const auto s1 = __builtin_amdgcn_permlane16_swap(o[i][1], o[i + 1][1], false, false);
        const v4u v = {s0[0], s1[0], s0[1], s1[1]};
        __builtin_amdgcn_raw_buffer_store_b128(v, rws, yo + (i >> 1) * 1024, 0, AUX_SC1);
      }
      rec_post(hown, 7);
      TS(2);
      WAIT_V(0);
      if (l == 0) __hip_atomic_store((GAS u32*)(ws + WS_FY) + FL * (4 * (c0 >> 1) + hw), tag, RLX_AGENT);
    }
    __builtin_amdgcn_s_setprio(0);
    if (b == 0 && tid == 0) __hip_atomic_store((GAS u32*)(ws + WS_NONCE), tag, RLX_AGENT);
    TS(7);
    {
      u32 st = st0.x;
      if (!all64(st0.y == tag)) st = poll_granule((const GAS u64*)(ws + s_st + sto), tag);
      lds_st<float>(M_RS + 64 * hw + 4 * i16, __builtin_amdgcn_rcpf(bf_hi(st)));
    }
    {
      const int pc = l & 7;
      const u32 yst = H_S + hw * 4160;
      u32 yl = WS_Y + (64 * dt + 16 * hw + (l >> 3)) * 4096 + (tt >> 3) * 1024 + (2 * (pc >> 2) + ((tt >> 2) & 1)) * 256 + (4 * (tt & 3) + (pc & 3)) * 16;
      u32 ysw = yst + (l >> 3) * 192 + 32 * (pc & 3) + 16 * (pc >> 2), ysr = yst + (i16 >> 2) * 192 + 32 * g + 8 * (l & 3), rw_ = H_U + l * 144 + 32 * hw;
      const GAS u32* fy = (const GAS u32*)(ws + WS_FY) + FL * (4 * (32 * dt + 8 * hw + (i16 >> 1)) + 2 * (l & 1) + (tt >> 4));
      PIN(yl); PIN(ysw); PIN(ysr); PIN(rw_); PIN(fy);
      REC_WAIT(hall, 7);
      rec_wait<2>(hown - (M_HF - M_CF), 3);
      __builtin_amdgcn_s_setprio(3);
      poll_flag(fy, tag);
      AFTER_POLL();
      rec_wait<2>(hown - (M_HF - M_CF), 4);
      TS(8);
      v4u yv[2];
      UNROLL for (int i = 0; i < 2; ++i) yv[i] = __builtin_amdgcn_raw_buffer_load_b128(rws, yl, i * 8 * 4096, AUX_SC1);
      UNROLL for (int i = 0; i < 2; ++i) lds_st<v4u>(ysw + 8 * i * 192, yv[i]);
      const v4u y0 = cat(lds_tr(ysr), lds_tr(ysr + 4 * 192)), y1 = cat(lds_tr(ysr + 8 * 192), lds_tr(ysr + 12 * 192));
      lds_st<v4u>(rw_, y0);
      lds_st<v4u>(rw_ + 16, y1);
    }
    rec_post(hown, 8);
    __builtin_amdgcn_s_setprio(0);
    TS(9);
    rec_spin(hall, 8);
    rec_wait<1>(hall - (M_HF - M_CF), 5);
    finish();
    asm volatile(".Lpf_end:");
  }
}

extern "C" void kernel_launch(void* const* d_in, const int* in_sizes, int n_in, void* d_out, int out_size, void* d_ws, size_t ws_size,
                              hipStream_t stream) {
  fused_s4d_mlp<<<256, 512, 0, stream>>>((const u16*)d_in[0], (const u16*)d_in[1], (const u16*)d_in[2], (const u16*)d_in[3], (const u16*)d_in[4],
                                         (const u16*)d_in[5], (const u16*)d_in[6], (const u16*)d_in[8], (const u16*)d_in[9], (u16*)d_out,
                                         (char*)d_ws);
}
```
